```python
import torch

def _softmax(x, axis=-1):
    return torch.softmax(x, dim=axis)
def _gelu(x):
    return torch.nn.functional.gelu(x, approximate="tanh")
def _silu(x):
    return torch.nn.functional.silu(x)
def _sigmoid(x):
    return torch.sigmoid(x)
def _layernorm(x, axis=-1, eps=1e-5):
    m = x.mean(dim=axis, keepdim=True)
    v = x.var(dim=axis, keepdim=True, unbiased=False)
    return (x - m) / torch.sqrt(v + eps)
def _scatter_add(tgt, idx, src, axis=0):
    return tgt.clone().index_add_(axis, idx, src)
def _segment_sum(src, seg, n):
    out = torch.zeros((n,) + src.shape[1:], dtype=src.dtype)
    return out.index_add_(0, seg, src)
def _pad(x, pw):
    flat = tuple(v for pair in reversed(pw) for v in pair)
    return torch.nn.functional.pad(x, flat)

kernel_name = 'recurrent_scan__compressed_kv_cla__dense_activation_gelu__decode'

def setup_inputs():

    _w = (lambda a: (lambda t: t.to(torch.bfloat16) if t.is_floating_point() else t)(torch.as_tensor(a)))
    def _A_setup():
        B, H, Sq, Sk, D, M = 4, 32, 1, 4096, 128, 512
        Dc = 4 * D
        return {
            "x": _w((lambda *s: torch.randn(*s, dtype=torch.bfloat16) * 0.1)(B, Sq, M)),
            "c_kv": _w((lambda *s: torch.randn(*s, dtype=torch.bfloat16) * 3.2)(B, Sk, Dc)),
            "w_q": _w((lambda *s: torch.randn(*s, dtype=torch.bfloat16) * 0.8)(M, H, D) * (M ** -0.5)),
            "w_uk": _w((lambda *s: torch.randn(*s, dtype=torch.bfloat16) * 0.8)(Dc, H, D) * (Dc ** -0.5)),
            "w_uv": _w((lambda *s: torch.randn(*s, dtype=torch.bfloat16) * 3.2)(Dc, H, D) * (Dc ** -0.5)),
            "w_o": _w((lambda *s: torch.randn(*s, dtype=torch.bfloat16) * 3.2)(H, D, M) * ((H * D) ** -0.5)),
        }
    _A_in = _A_setup()

    def _B_setup():
        B, S, D, F = 4, 1, 512, 1792
        return {
            "x": _w((lambda *s: torch.randn(*s, dtype=torch.bfloat16) * 0.1)(B, S, D)),
            "w1": _w((lambda *s: torch.randn(*s, dtype=torch.bfloat16) * 3.2)(D, F) * (D ** -0.5)),
            "b1": _w((lambda *s: torch.randn(*s, dtype=torch.bfloat16) * 3.2)(F) * 0.01),
            "w2": _w((lambda *s: torch.randn(*s, dtype=torch.bfloat16) * 3.2)(F, D) * (F ** -0.5)),
            "b2": _w((lambda *s: torch.randn(*s, dtype=torch.bfloat16) * 3.2)(D) * 0.01),
        }
    _B_in = _B_setup()

    return {
        "A_x": _A_in["x"],
        "A_c_kv": _A_in["c_kv"],
        "A_w_q": _A_in["w_q"],
        "A_w_uk": _A_in["w_uk"],
        "A_w_uv": _A_in["w_uv"],
        "A_w_o": _A_in["w_o"],
        "B_x": _B_in["x"],
        "B_w1": _B_in["w1"],
        "B_b1": _B_in["b1"],
        "B_w2": _B_in["w2"],
        "B_b2": _B_in["b2"],
    }

def reference(A_x, A_c_kv, A_w_q, A_w_uk, A_w_uv, A_w_o, B_x, B_w1, B_b1, B_w2, B_b2):
    def _A_ref(x, c_kv, w_q, w_uk, w_uv, w_o):
        B, Sq, M = x.shape[0], x.shape[1], x.shape[2]
        Sk = c_kv.shape[1]
        H, D = w_q.shape[1], w_q.shape[2]
        q = torch.einsum("bqm,mhd->bhqd", x, w_q)
        k = torch.einsum("bkc,chd->bhkd", c_kv, w_uk)
        v = torch.einsum("bkc,chd->bhkd", c_kv, w_uv)
        scores = torch.einsum("bhqd,bhkd->bhqk", q, k) * (D ** -0.5)
        off = Sk - Sq
        qi = torch.arange(Sq)[:, None]
        kj = torch.arange(Sk)[None, :]
        cmask = kj <= (qi + off)
        scores = torch.where(cmask[None, None], scores, -1e9)
        p = _softmax(scores, axis=-1)
        ctx = torch.einsum("bhqk,bhkd->bhqd", p, v)
        return torch.einsum("bhqd,hdm->bqm", ctx, w_o)

    def _B_ref(x, w1, b1, w2, b2):
        h = torch.einsum("bsd,df->bsf", x, w1) + b1
        h = _gelu(h)
        return torch.einsum("bsf,fd->bsd", h, w2) + b2

    x = A_x
    xn = _layernorm(x, axis=-1)
    s = _A_ref(xn, A_c_kv, A_w_q, A_w_uk, A_w_uv, A_w_o)
    g = _silu(_B_ref(xn, B_w1, B_b1, B_w2, B_b2))
    return x + s * g
```

```cpp
#include <hip/hip_runtime.h>
#include <hip/hip_bf16.h>

namespace mla {

typedef unsigned u32;
typedef unsigned long long u64;
typedef u32 v2u __attribute__((ext_vector_type(2)));
typedef u32 v4u __attribute__((ext_vector_type(4)));
typedef float f32x2 __attribute__((ext_vector_type(2)));
typedef float f32x4 __attribute__((ext_vector_type(4)));
typedef float f32x16 __attribute__((ext_vector_type(16)));
typedef short s16x4 __attribute__((ext_vector_type(4)));
typedef __bf16 bf16x2 __attribute__((ext_vector_type(2)));
typedef __bf16 bf16x8 __attribute__((ext_vector_type(8)));
#define LAS __attribute__((address_space(3)))
#define DI __device__ __forceinline__

constexpr int SC1 = 16;
constexpr u32 SENT = 0xFFFFFFFFu;
constexpr int SPIN_CAP = 8192;
constexpr int NFFN = 56;

constexpr int WS_EPOCH = 0;
constexpr int WS_FLAGW = 4096;
constexpr int WS_FLAGK = WS_FLAGW + 8192;
constexpr int WS_HINT1 = WS_FLAGK + 8192;
constexpr int WS_HINT2 = WS_HINT1 + 16384;
constexpr int WS_HINT3 = WS_HINT2 + 16384;
constexpr int WS_QG = 131072;
constexpr int WS_QA = WS_QG + 131072;
constexpr int WS_ML = WS_QA + 131072;
constexpr int WS_YP = WS_ML + 131072;
constexpr int WS_SP = 1048576;
constexpr int WS_OC = 2097152;
constexpr int WS_END = WS_OC + 4194304;
static_assert(WS_YP + 524288 == WS_SP, "workspace map");

#define Q_VMCNT 4
#define SLEEP_C 8
#define SLEEP_E 8

struct Params {
    const char* x; const char* ckv; const char* wq; const char* wuk; const char* wuv; const char* wo;
    const char* w1; const char* b1; const char* w2; const char* b2;
    char* out; char* ws;
};

#define STAMP(i) do {} while (0)
#define STAMP_DECL do {} while (0)
#define STAMP_OUT() do {} while (0)

template <int N> DI void wait_vm() { asm volatile("s_waitcnt vmcnt(%0)" :: "i"(N) : "memory"); }
DI void bar() { asm volatile("s_waitcnt lgkmcnt(0)\n\ts_barrier" ::: "memory"); }
DI void fence_compiler() { asm volatile("" ::: "memory"); }

DI void dma_nt(const char* base, u32 voff, u32 lds) {
    asm volatile("s_mov_b32 m0, %0\n\ts_nop 0\n\tglobal_load_lds_dwordx4 %1, %2 nt" :: "s"(lds), "v"(voff), "s"(base) : "memory");
}
DI void touch(const char* base, u32 voff, u32 lds) {
    asm volatile("s_mov_b32 m0, %0\n\ts_nop 0\n\tglobal_load_lds_dwordx4 %1, %2 sc1" :: "s"(lds), "v"(voff), "s"(base) : "memory");
}
DI v2u ld64_issue(const char* base, u32 voff) { v2u r; asm volatile("global_load_dwordx2 %0, %1, %2 sc1" : "=v"(r) : "v"(voff), "s"(base) : "memory"); return r; }
DI v4u ld128_issue(const char* base, u32 voff) { v4u r; asm volatile("global_load_dwordx4 %0, %1, %2" : "=v"(r) : "v"(voff), "s"(base) : "memory"); return r; }

typedef __amdgpu_buffer_rsrc_t rsrc_t;
DI v4u ldw128(rsrc_t rs, int off) { return __builtin_amdgcn_raw_buffer_load_b128(rs, off, 0, SC1); }
DI v2u ldw64(rsrc_t rs, int off) { return __builtin_amdgcn_raw_buffer_load_b64(rs, off, 0, SC1); }
DI u32 ldw32(rsrc_t rs, int off) { return __builtin_amdgcn_raw_buffer_load_b32(rs, off, 0, SC1); }
DI void stw128(rsrc_t rs, int off, v4u v) { __builtin_amdgcn_raw_buffer_store_b128(v, rs, off, 0, SC1); }
DI void stw64(rsrc_t rs, int off, v2u v) { __builtin_amdgcn_raw_buffer_store_b64(v, rs, off, 0, SC1); }

DI float as_f(u32 x) { return __builtin_bit_cast(float, x); }
DI u32 as_u(float x) { return __builtin_bit_cast(u32, x); }
DI float bf_lo(u32 x) { return as_f(x << 16); }
DI float bf_hi(u32 x) { return as_f(x & 0xFFFF0000u); }
DI u32 pk(float lo, float hi) { f32x2 f = {lo, hi}; return __builtin_bit_cast(u32, __builtin_convertvector(f, bf16x2)); }
DI f32x4 sel4(bool c, f32x4 a, f32x4 b) { return f32x4{c ? a.x : b.x, c ? a.y : b.y, c ? a.z : b.z, c ? a.w : b.w}; }
DI u32 umax4(v4u a) { return max(max(a.x, a.y), max(a.z, a.w)); }

template <int N> DI float ror(float x) { return as_f((u32)__builtin_amdgcn_update_dpp(0, (int)as_u(x), 0x120 + N, 0xf, 0xf, false)); }
DI float add16(float x) { auto r = __builtin_amdgcn_permlane16_swap(as_u(x), as_u(x), false, false); return as_f(r[0]) + as_f(r[1]); }
DI float add32(float x) { auto r = __builtin_amdgcn_permlane32_swap(as_u(x), as_u(x), false, false); return as_f(r[0]) + as_f(r[1]); }
DI float max16(float x) { auto r = __builtin_amdgcn_permlane16_swap(as_u(x), as_u(x), false, false); return fmaxf(as_f(r[0]), as_f(r[1])); }
DI float max32(float x) { auto r = __builtin_amdgcn_permlane32_swap(as_u(x), as_u(x), false, false); return fmaxf(as_f(r[0]), as_f(r[1])); }
DI float wave_sum(float x) { x += ror<8>(x); x += ror<4>(x); x += ror<2>(x); x += ror<1>(x); return add32(add16(x)); }

DI f32x4 mfma16(v4u a, v4u b, f32x4 c) { return __builtin_amdgcn_mfma_f32_16x16x32_bf16(__builtin_bit_cast(bf16x8, a), __builtin_bit_cast(bf16x8, b), c, 0, 0, 0); }
DI f32x16 mfma32(v4u a, v4u b, f32x16 c) { return __builtin_amdgcn_mfma_f32_32x32x16_bf16(__builtin_bit_cast(bf16x8, a), __builtin_bit_cast(bf16x8, b), c, 0, 0, 0); }

typedef LAS char* lds_t;
DI v4u lds128(lds_t L, u32 a) { return *(LAS v4u*)(L + a); }
DI v2u lds_tr(lds_t L, u32 a) { return __builtin_bit_cast(v2u, __builtin_amdgcn_ds_read_tr16_b64_v4i16((LAS s16x4*)(L + a))); }
DI v4u cat(v2u a, v2u b) { return v4u{a.x, a.y, b.x, b.y}; }

DI u32 swz(u32 row) { return ((row & 3) << 2) | ((row >> 2) & 3); }

template <int RB> DI v4u frag_tr(lds_t L, u32 base, u32 lane, u32 ks, u32 c0) {
    const u32 g = lane >> 4, q = (lane >> 2) & 3, p = lane & 3;
    const u32 r0 = 32 * ks + 8 * g + q, r1 = r0 + 4, ch = c0 + (p >> 1);
    return cat(lds_tr(L, base + r0 * RB + 16 * (ch ^ swz(r0)) + 8 * (p & 1)), lds_tr(L, base + r1 * RB + 16 * (ch ^ swz(r1)) + 8 * (p & 1)));
}

DI void wait_tag(rsrc_t rs, int off, v2u tag, bool active) {
    for (int n = 0; n < SPIN_CAP; ++n) {
        fence_compiler();
        bool ok = true;
        if (active) { const v2u f = ldw64(rs, off); ok = f.x == tag.x && f.y == tag.y; }
        if (__all(ok)) break;
        __builtin_amdgcn_s_sleep(1);
    }
    fence_compiler();
}

constexpr u32 XN_STRIDE = 1088, QS_STRIDE = 320;

DI void layernorm_to_lds(lds_t L, u32 xn_off, v4u xv, u32 w, u32 lane) {
    float f[8] = {bf_lo(xv.x), bf_hi(xv.x), bf_lo(xv.y), bf_hi(xv.y), bf_lo(xv.z), bf_hi(xv.z), bf_lo(xv.w), bf_hi(xv.w)};
    float s = ((f[0] + f[1]) + (f[2] + f[3])) + ((f[4] + f[5]) + (f[6] + f[7]));
    const float mean = wave_sum(s) * (1.0f / 512.0f);
    float v = 0.f;
#pragma unroll
    for (int i = 0; i < 8; ++i) { f[i] -= mean; v += f[i] * f[i]; }
    const float rstd = 1.0f / sqrtf(wave_sum(v) * (1.0f / 512.0f) + 1e-5f);
    *(LAS v4u*)(L + xn_off + w * XN_STRIDE + lane * 16) =
        v4u{pk(f[0] * rstd, f[1] * rstd), pk(f[2] * rstd, f[3] * rstd), pk(f[4] * rstd, f[5] * rstd), pk(f[6] * rstd, f[7] * rstd)};
}

DI void dma_slab(const char* base, u32 pitch, u32 lds, u32 w, u32 lane) {
    const u32 r = lane >> 2, ch = (lane & 3) ^ (((r >> 3) & 1) << 1);
#pragma unroll
    for (int i = 0; i < 8; ++i) dma_nt(base, (128 * w + 16 * i + r) * pitch + 16 * ch, lds + (128 * w + 16 * i) * 64);
}

DI void slab_product(lds_t L, u32 slab, u32 xn_off, u32 red_off, u32 w, u32 lane) {
    const u32 n = lane & 15, g = lane >> 4, q = (lane >> 2) & 3, p = lane & 3;
    f32x4 acc[2] = {{0.f, 0.f, 0.f, 0.f}, {0.f, 0.f, 0.f, 0.f}};
#pragma unroll
    for (int k = 0; k < 4; ++k) {
        const u32 ks = 4 * w + k;
        const v4u xb = lds128(L, xn_off + (n & 3) * XN_STRIDE + 64 * ks + 16 * g);
#pragma unroll
        for (int dt = 0; dt < 2; ++dt) {
            const u32 a0 = slab + (32 * ks + 8 * g + q) * 64 + 32 * (dt ^ (g & 1)) + 8 * p;
            acc[dt] = mfma16(cat(lds_tr(L, a0), lds_tr(L, a0 + 256)), xb, acc[dt]);
        }
    }
    if (n < 4) {
#pragma unroll
        for (int dt = 0; dt < 2; ++dt) *(LAS f32x4*)(L + red_off + (w * 4 + n) * 128 + 64 * dt + 16 * g) = acc[dt];
    }
}

DI void store_row_partials(rsrc_t rs, int region, int slots, int slot, const f32x4 (&acc)[8], u32 w, u32 lane) {
    const u32 n = lane & 15, g = lane >> 4, b = n & 3, k = n >> 2;
#pragma unroll
    for (int u = 0; u < 2; ++u) {
        const f32x4 v = sel4(k & 2, sel4(k & 1, acc[4 * u + 3], acc[4 * u + 2]), sel4(k & 1, acc[4 * u + 1], acc[4 * u]));
        const int f = 16 * b + 4 * w + 2 * u + (k >> 1);
        stw128(rs, region + (f * slots + slot) * 128 + 64 * (k & 1) + 16 * g, __builtin_bit_cast(v4u, v));
    }
}

DI void role_w(const Params& p, lds_t L, rsrc_t rs, u32 wi, u32 xcd) {
    STAMP_DECL;
    STAMP(0);
    const u32 tid = threadIdx.x, lane = tid & 63, w = __builtin_amdgcn_readfirstlane(tid >> 6);
    const u32 h = wi >> 2, j = wi & 3, n = lane & 15, g = lane >> 4;
    constexpr u32 L_WQ = 0, L_WUK = 32768, L_JUNK = 65536, L_WO = 0, L_HI = 131072;
    constexpr u32 L_XN = L_HI, L_RED = L_HI + 8192, L_QS = L_HI + 12288, L_CC = L_HI, L_CX = L_HI + 2048;

    v2u ev = ld64_issue(p.ws, WS_EPOCH);
    v4u xv = ld128_issue(p.x, tid * 16);
    if (w == 0) stw128(rs, WS_QA + ((g * 32 + h) * 4 + j) * 256 + n * 16, v4u{SENT, SENT, SENT, SENT});
    else if (w == 1) touch(p.ws, WS_QG + wi * 1024 + lane * 16, L_JUNK);
    else if (w == 2) touch(p.ws, WS_FLAGW + (lane & 7) * 1024 + (wi >> 1) * 16, L_JUNK + 1024);
    else touch(p.ws, WS_HINT3 + wi * 128 + (lane & 7) * 16, L_JUNK + 2048);
    dma_slab(p.wq + h * 256 + j * 64, 8192, L_WQ, w, lane);
#pragma unroll
    for (int i = 0; i < 8; ++i) {
        const u32 row = 32 * w + 4 * i + g;
        dma_nt(p.wuk + (128 * j) * 8192 + h * 256, row * 8192 + 16 * (n ^ (row & 15)), L_WUK + (32 * w + 4 * i) * 256);
    }
    asm volatile("s_waitcnt vmcnt(17)" : "+v"(ev), "+v"(xv) :: "memory");
    STAMP(1);
    const u64 e64 = (((u64)__builtin_amdgcn_readfirstlane(ev.y) << 32) | __builtin_amdgcn_readfirstlane(ev.x)) + 1;
    const v2u tag = {(u32)e64, (u32)(e64 >> 32)};
    layernorm_to_lds(L, L_XN, xv, w, lane);
    bar();
    wait_vm<8>();
    STAMP(2);
    if (w == 0 && lane < 8) stw64(rs, WS_FLAGW + lane * 1024 + wi * 8, tag);
    slab_product(L, L_WQ, L_XN, L_RED, w, lane);
    bar();
    if (lane < 32) {
        float s = 0.f;
#pragma unroll
        for (int ww = 0; ww < 4; ++ww) s += *(LAS float*)(L + L_RED + (ww * 4 + w) * 128 + lane * 4);
        stw64(rs, WS_QG + wi * 1024 + (w * 32 + lane) * 8, v2u{(pk(s, s) & 0xFFFFu) | (tag.x << 16), (u32)(e64 >> 16)});
    }
    STAMP(3);
    wait_vm<0>();
    v4u uk[2][4];
#pragma unroll
    for (int t = 0; t < 2; ++t)
#pragma unroll
        for (int ks = 0; ks < 4; ++ks) uk[t][ks] = lds128(L, L_WUK + (32 * w + 16 * t + n) * 256 + 16 * ((4 * ks + g) ^ n));
    STAMP(4);
    {
        v4u qg;
        for (int it = 0; it < SPIN_CAP; ++it) {
            fence_compiler();
            qg = ldw128(rs, WS_QG + (h * 4 + w) * 1024 + lane * 16);
            const u32 tl = tag.x & 0xFFFFu, th = (u32)(e64 >> 16);
            if (__all((qg.x >> 16) == tl && qg.y == th && (qg.z >> 16) == tl && qg.w == th)) break;
        }
        fence_compiler();
        *(LAS u32*)(L + L_QS + g * QS_STRIDE + 64 * w + 4 * n) = (qg.x & 0xFFFFu) | (qg.z << 16);
    }
    STAMP(5);
    bar();
    {
        f32x4 acc[2] = {{0.f, 0.f, 0.f, 0.f}, {0.f, 0.f, 0.f, 0.f}};
#pragma unroll
        for (int ks = 0; ks < 4; ++ks) {
            const v4u qb = lds128(L, L_QS + (n & 3) * QS_STRIDE + 64 * ks + 16 * g);
            acc[0] = mfma16(uk[0][ks], qb, acc[0]);
            acc[1] = mfma16(uk[1][ks], qb, acc[1]);
        }
        const float sc = 0.08838834764831845f * 1.4426950408889634f;
        const f32x4 v = sel4(n & 4, acc[1], acc[0]);
        if (n < 8) stw64(rs, WS_QA + (((n & 3) * 32 + h) * 4 + j) * 256 + 64 * w + 32 * (n >> 2) + 8 * g, v2u{pk(v.x * sc, v.y * sc), pk(v.z * sc, v.w * sc)});
    }
    STAMP(6);
    wait_tag(rs, WS_FLAGK + xcd * 1024 + (32 * w + (lane & 31)) * 8, tag, lane < 32);
    if (w == 0) wait_tag(rs, WS_HINT1 + wi * 128, tag, lane == 0);
    bar();
    STAMP(7);
#pragma unroll
    for (int i = 0; i < 8; ++i) {
        const u32 row = 32 * w + 4 * i + g;
        dma_nt(p.wuv + (128 * j) * 8192 + h * 256, row * 8192 + 16 * (n ^ swz(row)), L_HI + (32 * w + 4 * i) * 256);
    }
#pragma unroll
    for (int qq = 0; qq < 4; ++qq)
#pragma unroll
        for (int i = 0; i < 8; ++i) {
            const u32 row = 32 * ((j + qq) & 3) + 8 * w + i;
            dma_nt(p.wo + h * 131072, row * 1024 + 16 * (lane ^ swz(row)), L_WO + row * 1024);
        }
    wait_vm<32>();
    bar();
    v4u uv[2][4];
#pragma unroll
    for (int dt = 0; dt < 2; ++dt)
#pragma unroll
        for (int ks = 0; ks < 4; ++ks) uv[dt][ks] = frag_tr<256>(L, L_HI, lane, ks, 4 * w + 2 * dt);
    wait_vm<0>();
    bar();
    STAMP(8);
    v4u wo[8][4];
#pragma unroll
    for (int t = 0; t < 8; ++t)
#pragma unroll
        for (int ks = 0; ks < 4; ++ks) wo[t][ks] = frag_tr<1024>(L, L_WO, lane, ks, 16 * w + 2 * t);
    u32 dmask[4];
#pragma unroll
    for (int t = 0; t < 4; ++t) dmask[t] = (n >> 1) == (u32)t ? ((n & 1) ? 0xFFFF0000u : 0x0000FFFFu) : 0u;

    wait_tag(rs, WS_HINT2 + ((wi + 32 * w) & 127) * 128, tag, lane == 0);
    STAMP(9);
    __builtin_amdgcn_s_sleep(SLEEP_C);
    f32x4 cc = {0.f, 0.f, 0.f, 0.f};
    {
        v4u ml[4], oc[8], wa[8];
        for (int it = 0; it < SPIN_CAP; ++it) {
            fence_compiler();
#pragma unroll
            for (int i = 0; i < 4; ++i) ml[i] = ldw128(rs, WS_ML + wi * 1024 + w * 256 + g * 64 + 16 * i);
#pragma unroll
            for (int i = 0; i < 8; ++i) oc[i] = ldw128(rs, WS_OC + wi * 32768 + w * 8192 + i * 1024 + lane * 16);
            float mx = fmaxf(fmaxf(fmaxf(as_f(ml[0].x), as_f(ml[0].z)), fmaxf(as_f(ml[1].x), as_f(ml[1].z))),
                             fmaxf(fmaxf(as_f(ml[2].x), as_f(ml[2].z)), fmaxf(as_f(ml[3].x), as_f(ml[3].z))));
            mx = max32(max16(mx));
            float e[8], den = 0.f;
#pragma unroll
            for (int i = 0; i < 4; ++i) {
                e[2 * i] = __builtin_amdgcn_exp2f(as_f(ml[i].x) - mx);
                e[2 * i + 1] = __builtin_amdgcn_exp2f(as_f(ml[i].z) - mx);
                den += e[2 * i] * as_f(ml[i].y) + e[2 * i + 1] * as_f(ml[i].w);
            }
            const float inv = 1.0f / add32(add16(den));
#pragma unroll
            for (int i = 0; i < 8; ++i) {
                const u32 d = pk(e[i] * inv, e[i] * inv);
                wa[i] = v4u{d & dmask[0], d & dmask[1], d & dmask[2], d & dmask[3]};
            }
            u32 m = max(max(umax4(ml[0]), umax4(ml[1])), max(umax4(ml[2]), umax4(ml[3])));
#pragma unroll
            for (int i = 0; i < 8; ++i) m = max(m, umax4(oc[i]));
            if (__all(m != SENT)) break;
        }
        fence_compiler();
        STAMP(10);
        if (w == 0 && lane == 0) stw64(rs, WS_HINT3 + wi * 128, tag);
#pragma unroll
        for (int i = 0; i < 8; ++i) cc = mfma16(wa[i], oc[i], cc);
    }
    if (lane < 32) *(LAS v2u*)(L + L_CC + w * QS_STRIDE + 16 * n + 8 * g) = v2u{pk(cc.x, cc.y), pk(cc.z, cc.w)};
    bar();
    {
        f32x4 acc[2] = {{0.f, 0.f, 0.f, 0.f}, {0.f, 0.f, 0.f, 0.f}};
#pragma unroll
        for (int ks = 0; ks < 4; ++ks) {
            const v4u cb = lds128(L, L_CC + (n & 3) * QS_STRIDE + 64 * ks + 16 * g);
            acc[0] = mfma16(uv[0][ks], cb, acc[0]);
            acc[1] = mfma16(uv[1][ks], cb, acc[1]);
        }
        const f32x4 v = sel4(n & 4, acc[1], acc[0]);
        if (n < 8) *(LAS v2u*)(L + L_CX + (n & 3) * QS_STRIDE + 64 * w + 32 * (n >> 2) + 8 * g) = v2u{pk(v.x, v.y), pk(v.z, v.w)};
    }
    bar();
    {
        v4u cb[4];
#pragma unroll
        for (int ks = 0; ks < 4; ++ks) cb[ks] = lds128(L, L_CX + (n & 3) * QS_STRIDE + 64 * ks + 16 * g);
        f32x4 acc[8];
#pragma unroll
        for (int t = 0; t < 8; ++t) {
            acc[t] = f32x4{0.f, 0.f, 0.f, 0.f};
#pragma unroll
            for (int ks = 0; ks < 4; ++ks) acc[t] = mfma16(wo[t][ks], cb[ks], acc[t]);
        }
        store_row_partials(rs, WS_SP, 128, wi, acc, w, lane);
    }
    STAMP(11);
    STAMP_OUT();
}

template <int NI> DI void reduce_partials(rsrc_t rs, lds_t L, int off, u32 red_off, u32 w, u32 lane, bool last) {
    v4u a[NI];
    for (int it = 0; it < SPIN_CAP; ++it) {
        fence_compiler();
#pragma unroll
        for (int i = 0; i < NI; ++i) a[i] = ldw128(rs, off + ((i == NI - 1 && !last) ? 0 : i * 4096) + w * 1024 + lane * 16);
        u32 m = 0;
#pragma unroll
        for (int i = 0; i < NI; ++i) m = max(m, umax4(a[i]));
        if (__all(m != SENT)) break;
    }
    fence_compiler();
    f32x4 s = __builtin_bit_cast(f32x4, a[0]);
#pragma unroll
    for (int i = 1; i < NI; ++i)
        if (i < NI - 1 || last) s += __builtin_bit_cast(f32x4, a[i]);
#pragma unroll
    for (int e = 0; e < 4; ++e) s[e] = add32(add16(s[e] + ror<8>(s[e])));
    if (lane < 8) *(LAS f32x4*)(L + red_off + w * 128 + lane * 16) = s;
}

DI void role_k(const Params& p, lds_t L, rsrc_t rs, u32 ki, u32 xcd) {
    STAMP_DECL;
    STAMP(0);
    const u32 tid = threadIdx.x, lane = tid & 63, w = __builtin_amdgcn_readfirstlane(tid >> 6);
    const u32 b = ki >> 5, sp = ki & 31, n = lane & 15, g = lane >> 4;
    constexpr u32 L_C = 0, L_Q = 131072, L_P = L_Q, L_MX = L_Q + 8192, L_SL = L_Q + 8704;
    const v4u sent = {SENT, SENT, SENT, SENT};
    const int ml_off = WS_ML + b * 256 + (sp & 3) * 64 + (sp >> 2) * 8;

    v2u ev = ld64_issue(p.ws, WS_EPOCH);
#pragma unroll
    for (int i = 0; i < 8; ++i) stw128(rs, WS_OC + (32 * w + 4 * i + g) * 32768 + ki * 256 + n * 16, sent);
#pragma unroll
    for (int i = 0; i < 2; ++i) stw128(rs, WS_SP + ki * 8192 + i * 4096 + tid * 16, sent);
    stw128(rs, WS_YP + ki * 4096 + tid * 16, sent);
    if (lane < 32) stw64(rs, ml_off + (lane * 4 + w) * 1024, v2u{SENT, SENT});
    touch(p.ws, (w < 2 ? WS_HINT1 + w * 16384 + ki * 128 + (lane & 7) * 16 : WS_FLAGK + (lane & 7) * 1024 + (ki >> 1) * 16), L_Q + w * 1024);
    const char* cbase = p.ckv + (size_t)(b * 4096 + sp * 128) * 1024;
#define DMA_C(i) dma_nt(cbase, (32 * w + (i)) * 1024 + 16 * (lane ^ swz(i)), L_C + (32 * w + (i)) * 1024)
#pragma unroll
    for (int i = 0; i < 8; ++i) DMA_C(i);
    asm volatile("s_waitcnt vmcnt(8)" : "+v"(ev) :: "memory");
    bar();
    STAMP(1);
    const u64 e64 = (((u64)__builtin_amdgcn_readfirstlane(ev.y) << 32) | __builtin_amdgcn_readfirstlane(ev.x)) + 1;
    const v2u tag = {(u32)e64, (u32)(e64 >> 32)};
    if (lane < 2) stw64(rs, WS_FLAGK + (2 * w + lane) * 1024 + ki * 8, tag);
#pragma unroll
    for (int i = 8; i < 20; ++i) DMA_C(i);
    const int fw_off = WS_FLAGW + xcd * 1024 + (32 * w + (lane & 31)) * 8;
    v2u fw = ld64_issue(p.ws, fw_off);
#pragma unroll
    for (int i = 20; i < 32; ++i) DMA_C(i);
#undef DMA_C
    asm volatile("s_waitcnt vmcnt(12)" : "+v"(fw) :: "memory");
    if (!__all(fw.x == tag.x && fw.y == tag.y)) wait_tag(rs, fw_off, tag, true);
    if (w == 0 && lane == 0) stw64(rs, WS_HINT1 + ki * 128, tag);
    STAMP(2);
    wait_vm<Q_VMCNT>();
    STAMP(3);
    {
        v4u qv[8];
        for (int it = 0; it < SPIN_CAP; ++it) {
            fence_compiler();
#pragma unroll
            for (int i = 0; i < 8; ++i) qv[i] = ldw128(rs, WS_QA + (b * 32 + 8 * w + i) * 1024 + lane * 16);
            u32 m = 0;
#pragma unroll
            for (int i = 0; i < 8; ++i) m = max(m, umax4(qv[i]));
            if (__all(m != SENT)) break;
        }
        fence_compiler();
        STAMP(4);
#pragma unroll
        for (int i = 0; i < 8; ++i) *(LAS v4u*)(L + L_Q + (8 * w + i) * 1024 + 16 * (lane ^ swz(8 * w + i))) = qv[i];
    }
    wait_vm<0>();
    bar();
    STAMP(5);

    const u32 hd = lane & 31, hi = lane >> 5;
    f32x16 sa;
#pragma unroll
    for (int i = 0; i < 16; ++i) sa[i] = 0.f;
    {
        const u32 keyr = 16 * ((hd >> 2) & 1) + 4 * (hd >> 3) + (hd & 3);
        const u32 ab = L_C + (32 * w + keyr) * 1024, fa = swz(keyr), qb = L_Q + hd * 1024, fq = swz(hd);
#pragma unroll
        for (int s = 0; s < 32; ++s)
            sa = mfma32(lds128(L, ab + 16 * ((2 * s + hi) ^ fa)), lds128(L, qb + 16 * ((2 * s + hi) ^ fq)), sa);
    }
    float mloc = sa[0];
#pragma unroll
    for (int i = 1; i < 16; ++i) mloc = fmaxf(mloc, sa[i]);
    mloc = max32(mloc);
    bar();
    STAMP(6);
    if (lane < 32) *(LAS float*)(L + L_MX + w * 128 + lane * 4) = mloc;
    bar();
    const float mall = fmaxf(fmaxf(*(LAS float*)(L + L_MX + hd * 4), *(LAS float*)(L + L_MX + 128 + hd * 4)),
                             fmaxf(*(LAS float*)(L + L_MX + 256 + hd * 4), *(LAS float*)(L + L_MX + 384 + hd * 4)));
    {
        float ls = 0.f;
#pragma unroll
        for (int i = 0; i < 16; ++i) { sa[i] = __builtin_amdgcn_exp2f(sa[i] - mall); ls += sa[i]; }
#pragma unroll
        for (int c = 0; c < 2; ++c)
            *(LAS v4u*)(L + L_P + hd * 256 + 16 * ((4 * w + 2 * hi + c) ^ (hd & 15))) =
                v4u{pk(sa[8 * c], sa[8 * c + 1]), pk(sa[8 * c + 2], sa[8 * c + 3]), pk(sa[8 * c + 4], sa[8 * c + 5]), pk(sa[8 * c + 6], sa[8 * c + 7])};
        ls = add32(ls);
        if (lane < 32) *(LAS float*)(L + L_SL + w * 128 + lane * 4) = ls;
    }
    bar();
    STAMP(7);
    v4u pb[2][4];
#pragma unroll
    for (int ht = 0; ht < 2; ++ht)
#pragma unroll
        for (int ks = 0; ks < 4; ++ks) pb[ht][ks] = lds128(L, L_P + (16 * ht + n) * 256 + 16 * ((4 * ks + g) ^ n));
    if (lane < 32) {
        const float lsum = (*(LAS float*)(L + L_SL + lane * 4) + *(LAS float*)(L + L_SL + 128 + lane * 4)) +
                           (*(LAS float*)(L + L_SL + 256 + lane * 4) + *(LAS float*)(L + L_SL + 384 + lane * 4));
        stw64(rs, ml_off + (lane * 4 + w) * 1024, v2u{as_u(mall), as_u(lsum)});
    }
    if (w == 0 && lane == 0) stw64(rs, WS_HINT2 + ki * 128, tag);
#pragma unroll
    for (int u = 0; u < 4; ++u) {
        f32x4 acc[2][2];
#pragma unroll
        for (int e = 0; e < 2; ++e) {
            acc[e][0] = acc[e][1] = f32x4{0.f, 0.f, 0.f, 0.f};
#pragma unroll
            for (int ks = 0; ks < 4; ++ks) {
                const v4u a = frag_tr<1024>(L, L_C, lane, ks, 16 * w + 4 * u + 2 * e);
                acc[e][0] = mfma16(a, pb[0][ks], acc[e][0]);
                acc[e][1] = mfma16(a, pb[1][ks], acc[e][1]);
            }
        }
#pragma unroll
        for (int ht = 0; ht < 2; ++ht) {
            const auto r0 = __builtin_amdgcn_permlane16_swap(pk(acc[0][ht].x, acc[0][ht].y), pk(acc[1][ht].x, acc[1][ht].y), false, false);
            const auto r1 = __builtin_amdgcn_permlane16_swap(pk(acc[0][ht].z, acc[0][ht].w), pk(acc[1][ht].z, acc[1][ht].w), false, false);
            stw128(rs, WS_OC + ((16 * ht + n) * 4 + w) * 32768 + ki * 256 + 64 * u + 32 * (g & 1) + 16 * (g >> 1), v4u{r0[0], r1[0], r0[1], r1[1]});
        }
    }
    STAMP(8);

    const u32 f = ki >> 1;
    constexpr u32 L_W1 = 0, L_W2 = 32768, L_XN = 65536, L_RED = L_XN + 8192, L_HB = L_RED + 4096;
    if (ki & 1) {
        if (f < NFFN) {
            const v4u xv = *(const v4u*)(p.x + tid * 16);
            const float bias = lane < 32 ? bf_lo(*(const unsigned short*)(p.b1 + (32 * f + lane) * 2)) : 0.f;
            wait_tag(rs, WS_FLAGK + xcd * 1024 + (32 * w + (lane & 31)) * 8, tag, lane < 32);
            layernorm_to_lds(L, L_Q + 16384, xv, w, lane);
            bar();
            dma_slab(p.w1 + f * 64, 3584, L_W1, w, lane);
#pragma unroll
            for (int i = 0; i < 8; ++i) {
                const u32 row = 8 * w + i;
                dma_nt(p.w2 + (size_t)(32 * f) * 1024, row * 1024 + 16 * (lane ^ swz(row)), L_W2 + row * 1024);
            }
            wait_vm<8>();
            slab_product(L, L_W1, L_Q + 16384, L_RED, w, lane);
            wait_vm<0>();
            bar();
            STAMP(9);
            if (lane < 32) {
                float s = bias;
#pragma unroll
                for (int ww = 0; ww < 4; ++ww) s += *(LAS float*)(L + L_RED + (ww * 4 + w) * 128 + lane * 4);
                const float u = 0.7978845608028654f * (s + 0.044715f * s * s * s);
                const float th = 1.0f - 2.0f / (__expf(2.0f * u) + 1.0f);
                *(LAS unsigned short*)(L + L_HB + w * 64 + lane * 2) = (unsigned short)pk(0.5f * s * (1.0f + th), 0.f);
            }
            bar();
            const v4u hb = lds128(L, L_HB + (n & 3) * 64 + 16 * g);
            f32x4 acc[8];
#pragma unroll
            for (int t = 0; t < 8; ++t) acc[t] = mfma16(frag_tr<1024>(L, L_W2, lane, 0, 16 * w + 2 * t), hb, f32x4{0.f, 0.f, 0.f, 0.f});
            store_row_partials(rs, WS_YP, 64, f, acc, w, lane);
            STAMP(10);
        }
    } else {
        float xo = 0.f, bo = 0.f;
        if (tid < 32) {
            xo = bf_lo(*(const unsigned short*)(p.x + (32 * f + tid) * 2));
            bo = bf_lo(*(const unsigned short*)(p.b2 + (32 * (f & 15) + tid) * 2));
        }
        wait_tag(rs, WS_FLAGK + xcd * 1024 + (32 * w + (lane & 31)) * 8, tag, lane < 32);
        bar();
        for (int it = 0; it < SPIN_CAP; ++it) {
            fence_compiler();
            if (__builtin_amdgcn_readfirstlane(ldw32(rs, WS_YP + f * 8192 + ((f + 14 * w) % NFFN) * 128)) != SENT) break;
            __builtin_amdgcn_s_sleep(4);
        }
        reduce_partials<2>(rs, L, WS_YP + f * 8192, L_RED, w, lane, w < 3);
        bar();
        STAMP(9);
        float gate = 0.f;
        if (tid < 32) {
            const float y = bo + ((*(LAS float*)(L + L_RED + tid * 4) + *(LAS float*)(L + L_RED + 128 + tid * 4)) +
                                  (*(LAS float*)(L + L_RED + 256 + tid * 4) + *(LAS float*)(L + L_RED + 384 + tid * 4)));
            gate = y / (1.0f + __expf(-y));
        }
        wait_tag(rs, WS_HINT3 + ((ki + 32 * w) & 127) * 128, tag, lane == 0);
        STAMP(10);
        __builtin_amdgcn_s_sleep(SLEEP_E);
        reduce_partials<4>(rs, L, WS_SP + f * 16384, L_RED + 1024, w, lane, true);
        STAMP(11);
        bar();
        if (tid < 32) {
            const float s = (*(LAS float*)(L + L_RED + 1024 + tid * 4) + *(LAS float*)(L + L_RED + 1152 + tid * 4)) +
                            (*(LAS float*)(L + L_RED + 1280 + tid * 4) + *(LAS float*)(L + L_RED + 1408 + tid * 4));
            *(unsigned short*)(p.out + (32 * f + tid) * 2) = (unsigned short)pk(xo + s * gate, 0.f);
        }
        if (ki == 0 && tid == 0) stw64(rs, WS_EPOCH, tag);
        STAMP(12);
    }
    STAMP_OUT();
}

__global__ __launch_bounds__(256, 1) void mla_ffn_decode(Params p) {
    __shared__ __attribute__((aligned(16))) char smem[163840];
    lds_t L = (lds_t)smem;
    const rsrc_t rs = __builtin_amdgcn_make_buffer_rsrc(p.ws, 0, WS_END, 0x00020000);
    const u32 xcd = blockIdx.x & 7, r = blockIdx.x >> 3, idx = xcd * 16 + (r & 15);
    if (r < 16) role_w(p, L, rs, idx, xcd);
    else role_k(p, L, rs, idx, xcd);
}

}

extern "C" void kernel_launch(void* const* d_in, const int* in_sizes, int n_in,
                              void* d_out, int out_size, void* d_ws, size_t ws_size,
                              hipStream_t stream) {
    mla::Params p;
    p.x = (const char*)d_in[0];
    p.ckv = (const char*)d_in[1];
    p.wq = (const char*)d_in[2];
    p.wuk = (const char*)d_in[3];
    p.wuv = (const char*)d_in[4];
    p.wo = (const char*)d_in[5];
    p.w1 = (const char*)d_in[7];
    p.b1 = (const char*)d_in[8];
    p.w2 = (const char*)d_in[9];
    p.b2 = (const char*)d_in[10];
    p.out = (char*)d_out;
    p.ws = (char*)d_ws;
    mla::mla_ffn_decode<<<256, 256, 0, stream>>>(p);
}
```

```cpp
#include <hip/hip_runtime.h>
#include <hip/hip_bf16.h>

namespace mla {

typedef unsigned u32;
typedef unsigned long long u64;
typedef u32 v2u __attribute__((ext_vector_type(2)));
typedef u32 v4u __attribute__((ext_vector_type(4)));
typedef float f32x2 __attribute__((ext_vector_type(2)));
typedef float f32x4 __attribute__((ext_vector_type(4)));
typedef float f32x16 __attribute__((ext_vector_type(16)));
typedef short s16x4 __attribute__((ext_vector_type(4)));
typedef __bf16 bf16x2 __attribute__((ext_vector_type(2)));
typedef __bf16 bf16x8 __attribute__((ext_vector_type(8)));
#define LAS __attribute__((address_space(3)))
#define DI __device__ __forceinline__

constexpr int SC1 = 16;
constexpr u32 SENT = 0xFFFFFFFFu;
constexpr int SPIN_CAP = 8192;
constexpr int NFFN = 56;

constexpr int WS_EPOCH = 0;
constexpr int WS_FLAGW = 4096;
constexpr int WS_FLAGK = WS_FLAGW + 8192;
constexpr int WS_HINT1 = WS_FLAGK + 8192;
constexpr int WS_HINT2 = WS_HINT1 + 16384;
constexpr int WS_HINT3 = WS_HINT2 + 16384;
constexpr int WS_QG = 131072;
constexpr int WS_QA = WS_QG + 131072;
constexpr int WS_ML = WS_QA + 131072;
constexpr int WS_YP = WS_ML + 131072;
constexpr int WS_SP = 1048576;
constexpr int WS_OC = 2097152;
constexpr int WS_END = WS_OC + 4194304;
static_assert(WS_YP + 524288 == WS_SP, "workspace map");

#define Q_VMCNT 4
#define SLEEP_C 8
#define SLEEP_E 8

struct Params {
    const char* x; const char* ckv; const char* wq; const char* wuk; const char* wuv; const char* wo;
    const char* w1; const char* b1; const char* w2; const char* b2;
    char* out; char* ws;
};

#define STAMP(i) do {} while (0)
#define STAMP_DECL do {} while (0)
#define STAMP_OUT() do {} while (0)

template <int N> DI void wait_vm() { asm volatile("s_waitcnt vmcnt(%0)" :: "i"(N) : "memory"); }
DI void bar() { asm volatile("s_waitcnt lgkmcnt(0)\n\ts_barrier" ::: "memory"); }
DI void fence_compiler() { asm volatile("" ::: "memory"); }

DI void dma_nt(const char* base, u32 voff, u32 lds) {
    asm volatile("s_mov_b32 m0, %0\n\ts_nop 0\n\tglobal_load_lds_dwordx4 %1, %2 nt" :: "s"(lds), "v"(voff), "s"(base) : "memory");
}
DI void touch(const char* base, u32 voff, u32 lds) {
    asm volatile("s_mov_b32 m0, %0\n\ts_nop 0\n\tglobal_load_lds_dwordx4 %1, %2 sc1" :: "s"(lds), "v"(voff), "s"(base) : "memory");
}
DI v2u ld64_issue(const char* base, u32 voff) { v2u r; asm volatile("global_load_dwordx2 %0, %1, %2 sc1" : "=v"(r) : "v"(voff), "s"(base) : "memory"); return r; }
DI v4u ld128_issue(const char* base, u32 voff) { v4u r; asm volatile("global_load_dwordx4 %0, %1, %2" : "=v"(r) : "v"(voff), "s"(base) : "memory"); return r; }

typedef __amdgpu_buffer_rsrc_t rsrc_t;
DI v4u ldw128(rsrc_t rs, int off) { return __builtin_amdgcn_raw_buffer_load_b128(rs, off, 0, SC1); }
DI v2u ldw64(rsrc_t rs, int off) { return __builtin_amdgcn_raw_buffer_load_b64(rs, off, 0, SC1); }
DI u32 ldw32(rsrc_t rs, int off) { return __builtin_amdgcn_raw_buffer_load_b32(rs, off, 0, SC1); }
DI void stw128(rsrc_t rs, int off, v4u v) { __builtin_amdgcn_raw_buffer_store_b128(v, rs, off, 0, SC1); }
DI void stw64(rsrc_t rs, int off, v2u v) { __builtin_amdgcn_raw_buffer_store_b64(v, rs, off, 0, SC1); }

DI float as_f(u32 x) { return __builtin_bit_cast(float, x); }
DI u32 as_u(float x) { return __builtin_bit_cast(u32, x); }
DI float bf_lo(u32 x) { return as_f(x << 16); }
DI float bf_hi(u32 x) { return as_f(x & 0xFFFF0000u); }
DI u32 pk(float lo, float hi) { f32x2 f = {lo, hi}; return __builtin_bit_cast(u32, __builtin_convertvector(f, bf16x2)); }
DI f32x4 sel4(bool c, f32x4 a, f32x4 b) { return f32x4{c ? a.x : b.x, c ? a.y : b.y, c ? a.z : b.z, c ? a.w : b.w}; }
DI u32 umax4(v4u a) { return max(max(a.x, a.y), max(a.z, a.w)); }

template <int N> DI float ror(float x) { return as_f((u32)__builtin_amdgcn_update_dpp(0, (int)as_u(x), 0x120 + N, 0xf, 0xf, false)); }
DI float add16(float x) { auto r = __builtin_amdgcn_permlane16_swap(as_u(x), as_u(x), false, false); return as_f(r[0]) + as_f(r[1]); }
DI float add32(float x) { auto r = __builtin_amdgcn_permlane32_swap(as_u(x), as_u(x), false, false); return as_f(r[0]) + as_f(r[1]); }
DI float max16(float x) { auto r = __builtin_amdgcn_permlane16_swap(as_u(x), as_u(x), false, false); return fmaxf(as_f(r[0]), as_f(r[1])); }
DI float max32(float x) { auto r = __builtin_amdgcn_permlane32_swap(as_u(x), as_u(x), false, false); return fmaxf(as_f(r[0]), as_f(r[1])); }
DI float wave_sum(float x) { x += ror<8>(x); x += ror<4>(x); x += ror<2>(x); x += ror<1>(x); return add32(add16(x)); }

DI f32x4 mfma16(v4u a, v4u b, f32x4 c) { return __builtin_amdgcn_mfma_f32_16x16x32_bf16(__builtin_bit_cast(bf16x8, a), __builtin_bit_cast(bf16x8, b), c, 0, 0, 0); }
DI f32x16 mfma32(v4u a, v4u b, f32x16 c) { return __builtin_amdgcn_mfma_f32_32x32x16_bf16(__builtin_bit_cast(bf16x8, a), __builtin_bit_cast(bf16x8, b), c, 0, 0, 0); }

typedef LAS char* lds_t;
DI v4u lds128(lds_t L, u32 a) { return *(LAS v4u*)(L + a); }
DI v2u lds_tr(lds_t L, u32 a) { return __builtin_bit_cast(v2u, __builtin_amdgcn_ds_read_tr16_b64_v4i16((LAS s16x4*)(L + a))); }
DI v4u cat(v2u a, v2u b) { return v4u{a.x, a.y, b.x, b.y}; }

DI u32 swz(u32 row) { return ((row & 3) << 2) | ((row >> 2) & 3); }

template <int RB> DI v4u frag_tr(lds_t L, u32 base, u32 lane, u32 ks, u32 c0) {
    const u32 g = lane >> 4, q = (lane >> 2) & 3, p = lane & 3;
    const u32 r0 = 32 * ks + 8 * g + q, r1 = r0 + 4, ch = c0 + (p >> 1);
    return cat(lds_tr(L, base + r0 * RB + 16 * (ch ^ swz(r0)) + 8 * (p & 1)), lds_tr(L, base + r1 * RB + 16 * (ch ^ swz(r1)) + 8 * (p & 1)));
}

DI void wait_tag(rsrc_t rs, int off, v2u tag, bool active) {
    for (int n = 0; n < SPIN_CAP; ++n) {
        fence_compiler();
        bool ok = true;
        if (active) { const v2u f = ldw64(rs, off); ok = f.x == tag.x && f.y == tag.y; }
        if (__all(ok)) break;
        __builtin_amdgcn_s_sleep(1);
    }
    fence_compiler();
}

constexpr u32 XN_STRIDE = 1088, QS_STRIDE = 320;

DI void layernorm_to_lds(lds_t L, u32 xn_off, v4u xv, u32 w, u32 lane) {
    float f[8] = {bf_lo(xv.x), bf_hi(xv.x), bf_lo(xv.y), bf_hi(xv.y), bf_lo(xv.z), bf_hi(xv.z), bf_lo(xv.w), bf_hi(xv.w)};
    float s = ((f[0] + f[1]) + (f[2] + f[3])) + ((f[4] + f[5]) + (f[6] + f[7]));
    const float mean = wave_sum(s) * (1.0f / 512.0f);
    float v = 0.f;
#pragma unroll
    for (int i = 0; i < 8; ++i) { f[i] -= mean; v += f[i] * f[i]; }
    const float rstd = 1.0f / sqrtf(wave_sum(v) * (1.0f / 512.0f) + 1e-5f);
    *(LAS v4u*)(L + xn_off + w * XN_STRIDE + lane * 16) =
        v4u{pk(f[0] * rstd, f[1] * rstd), pk(f[2] * rstd, f[3] * rstd), pk(f[4] * rstd, f[5] * rstd), pk(f[6] * rstd, f[7] * rstd)};
}

DI void dma_slab(const char* base, u32 pitch, u32 lds, u32 w, u32 lane) {
    const u32 r = lane >> 2, ch = (lane & 3) ^ (((r >> 3) & 1) << 1);
#pragma unroll
    for (int i = 0; i < 8; ++i) dma_nt(base, (128 * w + 16 * i + r) * pitch + 16 * ch, lds + (128 * w + 16 * i) * 64);
}

DI void slab_product(lds_t L, u32 slab, u32 xn_off, u32 red_off, u32 w, u32 lane) {
    const u32 n = lane & 15, g = lane >> 4, q = (lane >> 2) & 3, p = lane & 3;
    f32x4 acc[2] = {{0.f, 0.f, 0.f, 0.f}, {0.f, 0.f, 0.f, 0.f}};
#pragma unroll
    for (int k = 0; k < 4; ++k) {
        const u32 ks = 4 * w + k;
        const v4u xb = lds128(L, xn_off + (n & 3) * XN_STRIDE + 64 * ks + 16 * g);
#pragma unroll
        for (int dt = 0; dt < 2; ++dt) {
            const u32 a0 = slab + (32 * ks + 8 * g + q) * 64 + 32 * (dt ^ (g & 1)) + 8 * p;
            acc[dt] = mfma16(cat(lds_tr(L, a0), lds_tr(L, a0 + 256)), xb, acc[dt]);
        }
    }
    if (n < 4) {
#pragma unroll
        for (int dt = 0; dt < 2; ++dt) *(LAS f32x4*)(L + red_off + (w * 4 + n) * 128 + 64 * dt + 16 * g) = acc[dt];
    }
}

DI void store_row_partials(rsrc_t rs, int region, int slots, int slot, const f32x4 (&acc)[8], u32 w, u32 lane) {
    const u32 n = lane & 15, g = lane >> 4, b = n & 3, k = n >> 2;
#pragma unroll
    for (int u = 0; u < 2; ++u) {
        const f32x4 v = sel4(k & 2, sel4(k & 1, acc[4 * u + 3], acc[4 * u + 2]), sel4(k & 1, acc[4 * u + 1], acc[4 * u]));
        const int f = 16 * b + 4 * w + 2 * u + (k >> 1);
        stw128(rs, region + (f * slots + slot) * 128 + 64 * (k & 1) + 16 * g, __builtin_bit_cast(v4u, v));
    }
}

DI void role_w(const Params& p, lds_t L, rsrc_t rs, u32 wi, u32 xcd) {
    STAMP_DECL;
    STAMP(0);
    const u32 tid = threadIdx.x, lane = tid & 63, w = __builtin_amdgcn_readfirstlane(tid >> 6);
    const u32 h = wi >> 2, j = wi & 3, n = lane & 15, g = lane >> 4;
    constexpr u32 L_WQ = 0, L_WUK = 32768, L_JUNK = 65536, L_WO = 0, L_HI = 131072;
    constexpr u32 L_XN = L_HI, L_RED = L_HI + 8192, L_QS = L_HI + 12288, L_CC = L_HI, L_CX = L_HI + 2048;

    v2u ev = ld64_issue(p.ws, WS_EPOCH);
    v4u xv = ld128_issue(p.x, tid * 16);
    if (w == 0) stw128(rs, WS_QA + ((g * 32 + h) * 4 + j) * 256 + n * 16, v4u{SENT, SENT, SENT, SENT});
    else if (w == 1) touch(p.ws, WS_QG + wi * 1024 + lane * 16, L_JUNK);
    else if (w == 2) touch(p.ws, WS_FLAGW + (lane & 7) * 1024 + (wi >> 1) * 16, L_JUNK + 1024);
    else touch(p.ws, WS_HINT3 + wi * 128 + (lane & 7) * 16, L_JUNK + 2048);
    dma_slab(p.wq + h * 256 + j * 64, 8192, L_WQ, w, lane);
#pragma unroll
    for (int i = 0; i < 8; ++i) {
        const u32 row = 32 * w + 4 * i + g;
        dma_nt(p.wuk + (128 * j) * 8192 + h * 256, row * 8192 + 16 * (n ^ (row & 15)), L_WUK + (32 * w + 4 * i) * 256);
    }
    asm volatile("s_waitcnt vmcnt(17)" : "+v"(ev), "+v"(xv) :: "memory");
    STAMP(1);
    const u64 e64 = (((u64)__builtin_amdgcn_readfirstlane(ev.y) << 32) | __builtin_amdgcn_readfirstlane(ev.x)) + 1;
    const v2u tag = {(u32)e64, (u32)(e64 >> 32)};
    layernorm_to_lds(L, L_XN, xv, w, lane);
    bar();
    wait_vm<8>();
    STAMP(2);
    if (w == 0 && lane < 8) stw64(rs, WS_FLAGW + lane * 1024 + wi * 8, tag);
    slab_product(L, L_WQ, L_XN, L_RED, w, lane);
    bar();
    if (lane < 32) {
        float s = 0.f;
#pragma unroll
        for (int ww = 0; ww < 4; ++ww) s += *(LAS float*)(L + L_RED + (ww * 4 + w) * 128 + lane * 4);
        stw64(rs, WS_QG + wi * 1024 + (w * 32 + lane) * 8, v2u{(pk(s, s) & 0xFFFFu) | (tag.x << 16), (u32)(e64 >> 16)});
    }
    STAMP(3);
    wait_vm<0>();
    v4u uk[2][4];
#pragma unroll
    for (int t = 0; t < 2; ++t)
#pragma unroll
        for (int ks = 0; ks < 4; ++ks) uk[t][ks] = lds128(L, L_WUK + (32 * w + 16 * t + n) * 256 + 16 * ((4 * ks + g) ^ n));
    STAMP(4);
    {
        v4u qg;
        for (int it = 0; it < SPIN_CAP; ++it) {
            fence_compiler();
            qg = ldw128(rs, WS_QG + (h * 4 + w) * 1024 + lane * 16);
            const u32 tl = tag.x & 0xFFFFu, th = (u32)(e64 >> 16);
            if (__all((qg.x >> 16) == tl && qg.y == th && (qg.z >> 16) == tl && qg.w == th)) break;
        }
        fence_compiler();
        *(LAS u32*)(L + L_QS + g * QS_STRIDE + 64 * w + 4 * n) = (qg.x & 0xFFFFu) | (qg.z << 16);
    }
    STAMP(5);
    bar();
    {
        f32x4 acc[2] = {{0.f, 0.f, 0.f, 0.f}, {0.f, 0.f, 0.f, 0.f}};
#pragma unroll
        for (int ks = 0; ks < 4; ++ks) {
            const v4u qb = lds128(L, L_QS + (n & 3) * QS_STRIDE + 64 * ks + 16 * g);
            acc[0] = mfma16(uk[0][ks], qb, acc[0]);
            acc[1] = mfma16(uk[1][ks], qb, acc[1]);
        }
        const float sc = 0.08838834764831845f * 1.4426950408889634f;
        const f32x4 v = sel4(n & 4, acc[1], acc[0]);
        if (n < 8) stw64(rs, WS_QA + (((n & 3) * 32 + h) * 4 + j) * 256 + 64 * w + 32 * (n >> 2) + 8 * g, v2u{pk(v.x * sc, v.y * sc), pk(v.z * sc, v.w * sc)});
    }
    STAMP(6);
    wait_tag(rs, WS_FLAGK + xcd * 1024 + (32 * w + (lane & 31)) * 8, tag, lane < 32);
    if (w == 0) wait_tag(rs, WS_HINT1 + wi * 128, tag, lane == 0);
    bar();
    STAMP(7);
#pragma unroll
    for (int i = 0; i < 8; ++i) {
        const u32 row = 32 * w + 4 * i + g;
        dma_nt(p.wuv + (128 * j) * 8192 + h * 256, row * 8192 + 16 * (n ^ swz(row)), L_HI + (32 * w + 4 * i) * 256);
    }
#pragma unroll
    for (int qq = 0; qq < 4; ++qq)
#pragma unroll
        for (int i = 0; i < 8; ++i) {
            const u32 row = 32 * ((j + qq) & 3) + 8 * w + i;
            dma_nt(p.wo + h * 131072, row * 1024 + 16 * (lane ^ swz(row)), L_WO + row * 1024);
        }
    wait_vm<32>();
    bar();
    v4u uv[2][4];
#pragma unroll
    for (int dt = 0; dt < 2; ++dt)
#pragma unroll
        for (int ks = 0; ks < 4; ++ks) uv[dt][ks] = frag_tr<256>(L, L_HI, lane, ks, 4 * w + 2 * dt);
    wait_vm<0>();
    bar();
    STAMP(8);
    v4u wo[8][4];
#pragma unroll
    for (int t = 0; t < 8; ++t)
#pragma unroll
        for (int ks = 0; ks < 4; ++ks) wo[t][ks] = frag_tr<1024>(L, L_WO, lane, ks, 16 * w + 2 * t);
    u32 dmask[4];
#pragma unroll
    for (int t = 0; t < 4; ++t) dmask[t] = (n >> 1) == (u32)t ? ((n & 1) ? 0xFFFF0000u : 0x0000FFFFu) : 0u;

    wait_tag(rs, WS_HINT2 + ((wi + 32 * w) & 127) * 128, tag, lane == 0);
    STAMP(9);
    __builtin_amdgcn_s_sleep(SLEEP_C);
    f32x4 cc = {0.f, 0.f, 0.f, 0.f};
    {
        v4u ml[4], oc[8], wa[8];
        for (int it = 0; it < SPIN_CAP; ++it) {
            fence_compiler();
#pragma unroll
            for (int i = 0; i < 4; ++i) ml[i] = ldw128(rs, WS_ML + wi * 1024 + w * 256 + g * 64 + 16 * i);
#pragma unroll
            for (int i = 0; i < 8; ++i) oc[i] = ldw128(rs, WS_OC + wi * 32768 + w * 8192 + i * 1024 + lane * 16);
            float mx = fmaxf(fmaxf(fmaxf(as_f(ml[0].x), as_f(ml[0].z)), fmaxf(as_f(ml[1].x), as_f(ml[1].z))),
                             fmaxf(fmaxf(as_f(ml[2].x), as_f(ml[2].z)), fmaxf(as_f(ml[3].x), as_f(ml[3].z))));
            mx = max32(max16(mx));
            float e[8], den = 0.f;
#pragma unroll
            for (int i = 0; i < 4; ++i) {
                e[2 * i] = __builtin_amdgcn_exp2f(as_f(ml[i].x) - mx);
                e[2 * i + 1] = __builtin_amdgcn_exp2f(as_f(ml[i].z) - mx);
                den += e[2 * i] * as_f(ml[i].y) + e[2 * i + 1] * as_f(ml[i].w);
            }
            const float inv = 1.0f / add32(add16(den));
#pragma unroll
            for (int i = 0; i < 8; ++i) {
                const u32 d = pk(e[i] * inv, e[i] * inv);
                wa[i] = v4u{d & dmask[0], d & dmask[1], d & dmask[2], d & dmask[3]};
            }
            u32 m = max(max(umax4(ml[0]), umax4(ml[1])), max(umax4(ml[2]), umax4(ml[3])));
#pragma unroll
            for (int i = 0; i < 8; ++i) m = max(m, umax4(oc[i]));
            if (__all(m != SENT)) break;
        }
        fence_compiler();
        STAMP(10);
        if (w == 0 && lane == 0) stw64(rs, WS_HINT3 + wi * 128, tag);
#pragma unroll
        for (int i = 0; i < 8; ++i) cc = mfma16(wa[i], oc[i], cc);
    }
    if (lane < 32) *(LAS v2u*)(L + L_CC + w * QS_STRIDE + 16 * n + 8 * g) = v2u{pk(cc.x, cc.y), pk(cc.z, cc.w)};
    bar();
    {
        f32x4 acc[2] = {{0.f, 0.f, 0.f, 0.f}, {0.f, 0.f, 0.f, 0.f}};
#pragma unroll
        for (int ks = 0; ks < 4; ++ks) {
            const v4u cb = lds128(L, L_CC + (n & 3) * QS_STRIDE + 64 * ks + 16 * g);
            acc[0] = mfma16(uv[0][ks], cb, acc[0]);
            acc[1] = mfma16(uv[1][ks], cb, acc[1]);
        }
        const f32x4 v = sel4(n & 4, acc[1], acc[0]);
        if (n < 8) *(LAS v2u*)(L + L_CX + (n & 3) * QS_STRIDE + 64 * w + 32 * (n >> 2) + 8 * g) = v2u{pk(v.x, v.y), pk(v.z, v.w)};
    }
    bar();
    {
        v4u cb[4];
#pragma unroll
        for (int ks = 0; ks < 4; ++ks) cb[ks] = lds128(L, L_CX + (n & 3) * QS_STRIDE + 64 * ks + 16 * g);
        f32x4 acc[8];
#pragma unroll
        for (int t = 0; t < 8; ++t) {
            acc[t] = f32x4{0.f, 0.f, 0.f, 0.f};
#pragma unroll
            for (int ks = 0; ks < 4; ++ks) acc[t] = mfma16(wo[t][ks], cb[ks], acc[t]);
        }
        store_row_partials(rs, WS_SP, 128, wi, acc, w, lane);
    }
    STAMP(11);
    STAMP_OUT();
}

template <int NI> DI void reduce_partials(rsrc_t rs, lds_t L, int off, u32 red_off, u32 w, u32 lane, bool last) {
    v4u a[NI];
    for (int it = 0; it < SPIN_CAP; ++it) {
        fence_compiler();
#pragma unroll
        for (int i = 0; i < NI; ++i) a[i] = ldw128(rs, off + ((i == NI - 1 && !last) ? 0 : i * 4096) + w * 1024 + lane * 16);
        u32 m = 0;
#pragma unroll
        for (int i = 0; i < NI; ++i) m = max(m, umax4(a[i]));
        if (__all(m != SENT)) break;
    }
    fence_compiler();
    f32x4 s = __builtin_bit_cast(f32x4, a[0]);
#pragma unroll
    for (int i = 1; i < NI; ++i)
        if (i < NI - 1 || last) s += __builtin_bit_cast(f32x4, a[i]);
#pragma unroll
    for (int e = 0; e < 4; ++e) s[e] = add32(add16(s[e] + ror<8>(s[e])));
    if (lane < 8) *(LAS f32x4*)(L + red_off + w * 128 + lane * 16) = s;
}

DI void role_k(const Params& p, lds_t L, rsrc_t rs, u32 ki, u32 xcd, u64 c0) {
    STAMP_DECL;
    STAMP(0);
    const u32 tid = threadIdx.x, lane = tid & 63, w = __builtin_amdgcn_readfirstlane(tid >> 6);
    const u32 b = ki >> 5, sp = ki & 31, n = lane & 15, g = lane >> 4;
    constexpr u32 L_C = 0, L_Q = 131072, L_P = L_Q, L_MX = L_Q + 8192, L_SL = L_Q + 8704;
    const v4u sent = {SENT, SENT, SENT, SENT};
    const int ml_off = WS_ML + b * 256 + (sp & 3) * 64 + (sp >> 2) * 8;

    v2u ev = ld64_issue(p.ws, WS_EPOCH);
#pragma unroll
    for (int i = 0; i < 8; ++i) stw128(rs, WS_OC + (32 * w + 4 * i + g) * 32768 + ki * 256 + n * 16, sent);
#pragma unroll
    for (int i = 0; i < 2; ++i) stw128(rs, WS_SP + ki * 8192 + i * 4096 + tid * 16, sent);
    stw128(rs, WS_YP + ki * 4096 + tid * 16, sent);
    if (lane < 32) stw64(rs, ml_off + (lane * 4 + w) * 1024, v2u{SENT, SENT});
    touch(p.ws, (w < 2 ? WS_HINT1 + w * 16384 + ki * 128 + (lane & 7) * 16 : WS_FLAGK + (lane & 7) * 1024 + (ki >> 1) * 16), L_Q + w * 1024);
    const char* cbase = p.ckv + (size_t)(b * 4096 + sp * 128) * 1024;
#define DMA_C(i) dma_nt(cbase, (32 * w + (i)) * 1024 + 16 * (lane ^ swz(i)), L_C + (32 * w + (i)) * 1024)
#pragma unroll
    for (int i = 0; i < 8; ++i) DMA_C(i);
    asm volatile("s_waitcnt vmcnt(8)" : "+v"(ev) :: "memory");
    bar();
    STAMP(1);
    const u64 e64 = (((u64)__builtin_amdgcn_readfirstlane(ev.y) << 32) | __builtin_amdgcn_readfirstlane(ev.x)) + 1;
    const v2u tag = {(u32)e64, (u32)(e64 >> 32)};
    if (lane < 2) stw64(rs, WS_FLAGK + (2 * w + lane) * 1024 + ki * 8, tag);
#pragma unroll
    for (int i = 8; i < 20; ++i) DMA_C(i);
    const int fw_off = WS_FLAGW + xcd * 1024 + (32 * w + (lane & 31)) * 8;
    v2u fw = ld64_issue(p.ws, fw_off);
#pragma unroll
    for (int i = 20; i < 32; ++i) DMA_C(i);
#undef DMA_C
    asm volatile("s_waitcnt vmcnt(12)" : "+v"(fw) :: "memory");
    if (!__all(fw.x == tag.x && fw.y == tag.y)) wait_tag(rs, fw_off, tag, true);
    if (w == 0 && lane == 0) stw64(rs, WS_HINT1 + ki * 128, tag);
    STAMP(2);
    wait_vm<Q_VMCNT>();
    STAMP(3);
    {
        v4u qv[8];
        for (int it = 0; it < SPIN_CAP; ++it) {
            fence_compiler();
#pragma unroll
            for (int i = 0; i < 8; ++i) qv[i] = ldw128(rs, WS_QA + (b * 32 + 8 * w + i) * 1024 + lane * 16);
            u32 m = 0;
#pragma unroll
            for (int i = 0; i < 8; ++i) m = max(m, umax4(qv[i]));
            if (__all(m != SENT)) break;
        }
        fence_compiler();
        STAMP(4);
#pragma unroll
        for (int i = 0; i < 8; ++i) *(LAS v4u*)(L + L_Q + (8 * w + i) * 1024 + 16 * (lane ^ swz(8 * w + i))) = qv[i];
    }
    wait_vm<0>();
    bar();
    STAMP(5);

    const u32 hd = lane & 31, hi = lane >> 5;
    f32x16 sa;
#pragma unroll
    for (int i = 0; i < 16; ++i) sa[i] = 0.f;
    {
        const u32 keyr = 16 * ((hd >> 2) & 1) + 4 * (hd >> 3) + (hd & 3);
        const u32 ab = L_C + (32 * w + keyr) * 1024, fa = swz(keyr), qb = L_Q + hd * 1024, fq = swz(hd);
#pragma unroll
        for (int s = 0; s < 32; ++s)
            sa = mfma32(lds128(L, ab + 16 * ((2 * s + hi) ^ fa)), lds128(L, qb + 16 * ((2 * s + hi) ^ fq)), sa);
    }
    float mloc = sa[0];
#pragma unroll
    for (int i = 1; i < 16; ++i) mloc = fmaxf(mloc, sa[i]);
    mloc = max32(mloc);
    bar();
    STAMP(6);
    if (lane < 32) *(LAS float*)(L + L_MX + w * 128 + lane * 4) = mloc;
    bar();
    const float mall = fmaxf(fmaxf(*(LAS float*)(L + L_MX + hd * 4), *(LAS float*)(L + L_MX + 128 + hd * 4)),
                             fmaxf(*(LAS float*)(L + L_MX + 256 + hd * 4), *(LAS float*)(L + L_MX + 384 + hd * 4)));
    {
        float ls = 0.f;
#pragma unroll
        for (int i = 0; i < 16; ++i) { sa[i] = __builtin_amdgcn_exp2f(sa[i] - mall); ls += sa[i]; }
#pragma unroll
        for (int c = 0; c < 2; ++c)
            *(LAS v4u*)(L + L_P + hd * 256 + 16 * ((4 * w + 2 * hi + c) ^ (hd & 15))) =
                v4u{pk(sa[8 * c], sa[8 * c + 1]), pk(sa[8 * c + 2], sa[8 * c + 3]), pk(sa[8 * c + 4], sa[8 * c + 5]), pk(sa[8 * c + 6], sa[8 * c + 7])};
        ls = add32(ls);
        if (lane < 32) *(LAS float*)(L + L_SL + w * 128 + lane * 4) = ls;
    }
    bar();
    STAMP(7);
    v4u pb[2][4];
#pragma unroll
    for (int ht = 0; ht < 2; ++ht)
#pragma unroll
        for (int ks = 0; ks < 4; ++ks) pb[ht][ks] = lds128(L, L_P + (16 * ht + n) * 256 + 16 * ((4 * ks + g) ^ n));
    if (lane < 32) {
        const float lsum = (*(LAS float*)(L + L_SL + lane * 4) + *(LAS float*)(L + L_SL + 128 + lane * 4)) +
                           (*(LAS float*)(L + L_SL + 256 + lane * 4) + *(LAS float*)(L + L_SL + 384 + lane * 4));
        stw64(rs, ml_off + (lane * 4 + w) * 1024, v2u{as_u(mall), as_u(lsum)});
    }
    if (w == 0 && lane == 0) stw64(rs, WS_HINT2 + ki * 128, tag);
#pragma unroll
    for (int u = 0; u < 4; ++u) {
        f32x4 acc[2][2];
#pragma unroll
        for (int e = 0; e < 2; ++e) {
            acc[e][0] = acc[e][1] = f32x4{0.f, 0.f, 0.f, 0.f};
#pragma unroll
            for (int ks = 0; ks < 4; ++ks) {
                const v4u a = frag_tr<1024>(L, L_C, lane, ks, 16 * w + 4 * u + 2 * e);
                acc[e][0] = mfma16(a, pb[0][ks], acc[e][0]);
                acc[e][1] = mfma16(a, pb[1][ks], acc[e][1]);
            }
        }
#pragma unroll
        for (int ht = 0; ht < 2; ++ht) {
            const auto r0 = __builtin_amdgcn_permlane16_swap(pk(acc[0][ht].x, acc[0][ht].y), pk(acc[1][ht].x, acc[1][ht].y), false, false);
            const auto r1 = __builtin_amdgcn_permlane16_swap(pk(acc[0][ht].z, acc[0][ht].w), pk(acc[1][ht].z, acc[1][ht].w), false, false);
            stw128(rs, WS_OC + ((16 * ht + n) * 4 + w) * 32768 + ki * 256 + 64 * u + 32 * (g & 1) + 16 * (g >> 1), v4u{r0[0], r1[0], r0[1], r1[1]});
        }
    }
    STAMP(8);

    const u32 f = ki >> 1;
    constexpr u32 L_W1 = 0, L_W2 = 32768, L_XN = 65536, L_RED = L_XN + 8192, L_HB = L_RED + 4096;
    if (ki & 1) {
        if (f < NFFN) {
            const v4u xv = *(const v4u*)(p.x + tid * 16);
            const float bias = lane < 32 ? bf_lo(*(const unsigned short*)(p.b1 + (32 * f + lane) * 2)) : 0.f;
            wait_tag(rs, WS_FLAGK + xcd * 1024 + (32 * w + (lane & 31)) * 8, tag, lane < 32);
            layernorm_to_lds(L, L_Q + 16384, xv, w, lane);
            bar();
            dma_slab(p.w1 + f * 64, 3584, L_W1, w, lane);
#pragma unroll
            for (int i = 0; i < 8; ++i) {
                const u32 row = 8 * w + i;
                dma_nt(p.w2 + (size_t)(32 * f) * 1024, row * 1024 + 16 * (lane ^ swz(row)), L_W2 + row * 1024);
            }
            wait_vm<8>();
            slab_product(L, L_W1, L_Q + 16384, L_RED, w, lane);
            wait_vm<0>();
            bar();
            STAMP(9);
            if (lane < 32) {
                float s = bias;
#pragma unroll
                for (int ww = 0; ww < 4; ++ww) s += *(LAS float*)(L + L_RED + (ww * 4 + w) * 128 + lane * 4);
                const float u = 0.7978845608028654f * (s + 0.044715f * s * s * s);
                const float th = 1.0f - 2.0f / (__expf(2.0f * u) + 1.0f);
                *(LAS unsigned short*)(L + L_HB + w * 64 + lane * 2) = (unsigned short)pk(0.5f * s * (1.0f + th), 0.f);
            }
            bar();
            const v4u hb = lds128(L, L_HB + (n & 3) * 64 + 16 * g);
            f32x4 acc[8];
#pragma unroll
            for (int t = 0; t < 8; ++t) acc[t] = mfma16(frag_tr<1024>(L, L_W2, lane, 0, 16 * w + 2 * t), hb, f32x4{0.f, 0.f, 0.f, 0.f});
            store_row_partials(rs, WS_YP, 64, f, acc, w, lane);
            STAMP(10);
        }
    } else {
        float xo = 0.f, bo = 0.f;
        if (tid < 32) {
            xo = bf_lo(*(const unsigned short*)(p.x + (32 * f + tid) * 2));
            bo = bf_lo(*(const unsigned short*)(p.b2 + (32 * (f & 15) + tid) * 2));
        }
        wait_tag(rs, WS_FLAGK + xcd * 1024 + (32 * w + (lane & 31)) * 8, tag, lane < 32);
        bar();
        for (int it = 0; it < SPIN_CAP; ++it) {
            fence_compiler();
            if (__builtin_amdgcn_readfirstlane(ldw32(rs, WS_YP + f * 8192 + ((f + 14 * w) % NFFN) * 128)) != SENT) break;
            __builtin_amdgcn_s_sleep(4);
        }
        reduce_partials<2>(rs, L, WS_YP + f * 8192, L_RED, w, lane, w < 3);
        bar();
        STAMP(9);
        float gate = 0.f;
        if (tid < 32) {
            const float y = bo + ((*(LAS float*)(L + L_RED + tid * 4) + *(LAS float*)(L + L_RED + 128 + tid * 4)) +
                                  (*(LAS float*)(L + L_RED + 256 + tid * 4) + *(LAS float*)(L + L_RED + 384 + tid * 4)));
            gate = y / (1.0f + __expf(-y));
        }
        wait_tag(rs, WS_HINT3 + ((ki + 32 * w) & 127) * 128, tag, lane == 0);
        STAMP(10);
        __builtin_amdgcn_s_sleep(SLEEP_E);
        reduce_partials<4>(rs, L, WS_SP + f * 16384, L_RED + 1024, w, lane, true);
        STAMP(11);
        bar();
        if (tid < 32) {
            const float s = (*(LAS float*)(L + L_RED + 1024 + tid * 4) + *(LAS float*)(L + L_RED + 1152 + tid * 4)) +
                            (*(LAS float*)(L + L_RED + 1280 + tid * 4) + *(LAS float*)(L + L_RED + 1408 + tid * 4));
            *(unsigned short*)(p.out + (32 * f + tid) * 2) = (unsigned short)pk(xo + s * gate, 0.f);
        }
        if (ki == 0 && tid == 0) stw64(rs, WS_EPOCH, tag);
        if (ki == 0) { u64 c1;
            for (int i = 0; i < 1000000; ++i) { asm volatile("s_memrealtime %0\n\ts_waitcnt lgkmcnt(0)" : "=s"(c1) :: "memory"); if (c1 - c0 > 5000) break; } }
        STAMP(12);
    }
    STAMP_OUT();
}

__global__ __launch_bounds__(256, 1) void mla_ffn_decode(Params p_) {
    __shared__ __attribute__((aligned(16))) char smem[163840];
    u64 c0; asm volatile("s_memrealtime %0\n\ts_waitcnt lgkmcnt(0)" : "=s"(c0) :: "memory");
    const __attribute__((address_space(4))) Params* pp = (const __attribute__((address_space(4))) Params*)__builtin_amdgcn_kernarg_segment_ptr();
    asm volatile("" : "+s"(pp) :: "memory");
    Params p;
    p.x = pp->x; p.ckv = pp->ckv; p.wq = pp->wq; p.wuk = pp->wuk; p.wuv = pp->wuv; p.wo = pp->wo; p.w1 = pp->w1; p.b1 = pp->b1; p.w2 = pp->w2; p.b2 = pp->b2; p.out = pp->out; p.ws = pp->ws;
    lds_t L = (lds_t)smem;
    const rsrc_t rs = __builtin_amdgcn_make_buffer_rsrc(p.ws, 0, WS_END, 0x00020000);
    const u32 xcd = blockIdx.x & 7, r = blockIdx.x >> 3, idx = xcd * 16 + (r & 15);
    if (r < 16) role_w(p, L, rs, idx, xcd);
    else role_k(p, L, rs, idx, xcd, c0);
}

}

extern "C" void kernel_launch(void* const* d_in, const int* in_sizes, int n_in,
                              void* d_out, int out_size, void* d_ws, size_t ws_size,
                              hipStream_t stream) {
    mla::Params p;
    p.x = (const char*)d_in[0];
    p.ckv = (const char*)d_in[1];
    p.wq = (const char*)d_in[2];
    p.wuk = (const char*)d_in[3];
    p.wuv = (const char*)d_in[4];
    p.wo = (const char*)d_in[5];
    p.w1 = (const char*)d_in[7];
    p.b1 = (const char*)d_in[8];
    p.w2 = (const char*)d_in[9];
    p.b2 = (const char*)d_in[10];
    p.out = (char*)d_out;
    p.ws = (char*)d_ws;
    mla::mla_ffn_decode<<<256, 256, 0, stream>>>(p);
}
```

```cpp
#include <hip/hip_runtime.h>
#include <hip/hip_bf16.h>

namespace mla {

typedef unsigned u32;
typedef unsigned long long u64;
typedef u32 v2u __attribute__((ext_vector_type(2)));
typedef u32 v4u __attribute__((ext_vector_type(4)));
typedef float f32x2 __attribute__((ext_vector_type(2)));
typedef float f32x4 __attribute__((ext_vector_type(4)));
typedef float f32x16 __attribute__((ext_vector_type(16)));
typedef short s16x4 __attribute__((ext_vector_type(4)));
typedef __bf16 bf16x2 __attribute__((ext_vector_type(2)));
typedef __bf16 bf16x8 __attribute__((ext_vector_type(8)));
#define LAS __attribute__((address_space(3)))
#define DI __device__ __forceinline__

constexpr int SC1 = 16;
constexpr u32 SENT = 0xFFFFFFFFu;
constexpr int SPIN_CAP = 8192;
constexpr int NFFN = 56;

constexpr int WS_EPOCH = 0;
constexpr int WS_FLAGW = 4096;
constexpr int WS_FLAGK = WS_FLAGW + 8192;
constexpr int WS_HINT1 = WS_FLAGK + 8192;
constexpr int WS_HINT2 = WS_HINT1 + 16384;
constexpr int WS_HINT3 = WS_HINT2 + 16384;
constexpr int WS_QG = 131072;
constexpr int WS_QA = WS_QG + 131072;
constexpr int WS_ML = WS_QA + 131072;
constexpr int WS_YP = WS_ML + 131072;
constexpr int WS_SP = 1048576;
constexpr int WS_OC = 2097152;
constexpr int WS_END = WS_OC + 4194304;
static_assert(WS_YP + 524288 == WS_SP, "workspace map");

#define Q_VMCNT 4
#define SLEEP_C 8
#define SLEEP_E 8

struct Params {
    const char* x; const char* ckv; const char* wq; const char* wuk; const char* wuv; const char* wo;
    const char* w1; const char* b1; const char* w2; const char* b2;
    char* out; char* ws;
};

#define STAMP(i) do {} while (0)
#define STAMP_DECL do {} while (0)
#define STAMP_OUT() do {} while (0)

template <int N> DI void wait_vm() { asm volatile("s_waitcnt vmcnt(%0)" :: "i"(N) : "memory"); }
DI void bar() { asm volatile("s_waitcnt lgkmcnt(0)\n\ts_barrier" ::: "memory"); }
DI void fence_compiler() { asm volatile("" ::: "memory"); }

DI void dma_nt(const char* base, u32 voff, u32 lds) {
    asm volatile("s_mov_b32 m0, %0\n\ts_nop 0\n\tglobal_load_lds_dwordx4 %1, %2 nt" :: "s"(lds), "v"(voff), "s"(base) : "memory");
}
DI void touch(const char* base, u32 voff, u32 lds) {
    asm volatile("s_mov_b32 m0, %0\n\ts_nop 0\n\tglobal_load_lds_dwordx4 %1, %2 sc1" :: "s"(lds), "v"(voff), "s"(base) : "memory");
}
DI v2u ld64_issue(const char* base, u32 voff) { v2u r; asm volatile("global_load_dwordx2 %0, %1, %2 sc1" : "=v"(r) : "v"(voff), "s"(base) : "memory"); return r; }
DI v4u ld128_issue(const char* base, u32 voff) { v4u r; asm volatile("global_load_dwordx4 %0, %1, %2" : "=v"(r) : "v"(voff), "s"(base) : "memory"); return r; }

typedef __amdgpu_buffer_rsrc_t rsrc_t;
DI v4u ldw128(rsrc_t rs, int off) { return __builtin_amdgcn_raw_buffer_load_b128(rs, off, 0, SC1); }
DI v2u ldw64(rsrc_t rs, int off) { return __builtin_amdgcn_raw_buffer_load_b64(rs, off, 0, SC1); }
DI u32 ldw32(rsrc_t rs, int off) { return __builtin_amdgcn_raw_buffer_load_b32(rs, off, 0, SC1); }
DI void stw128(rsrc_t rs, int off, v4u v) { __builtin_amdgcn_raw_buffer_store_b128(v, rs, off, 0, SC1); }
DI void stw64(rsrc_t rs, int off, v2u v) { __builtin_amdgcn_raw_buffer_store_b64(v, rs, off, 0, SC1); }

DI float as_f(u32 x) { return __builtin_bit_cast(float, x); }
DI u32 as_u(float x) { return __builtin_bit_cast(u32, x); }
DI float bf_lo(u32 x) { return as_f(x << 16); }
DI float bf_hi(u32 x) { return as_f(x & 0xFFFF0000u); }
DI u32 pk(float lo, float hi) { f32x2 f = {lo, hi}; return __builtin_bit_cast(u32, __builtin_convertvector(f, bf16x2)); }
DI f32x4 sel4(bool c, f32x4 a, f32x4 b) { return f32x4{c ? a.x : b.x, c ? a.y : b.y, c ? a.z : b.z, c ? a.w : b.w}; }
DI u32 umax4(v4u a) { return max(max(a.x, a.y), max(a.z, a.w)); }

template <int N> DI float ror(float x) { return as_f((u32)__builtin_amdgcn_update_dpp(0, (int)as_u(x), 0x120 + N, 0xf, 0xf, false)); }
DI float add16(float x) { auto r = __builtin_amdgcn_permlane16_swap(as_u(x), as_u(x), false, false); return as_f(r[0]) + as_f(r[1]); }
DI float add32(float x) { auto r = __builtin_amdgcn_permlane32_swap(as_u(x), as_u(x), false, false); return as_f(r[0]) + as_f(r[1]); }
DI float max16(float x) { auto r = __builtin_amdgcn_permlane16_swap(as_u(x), as_u(x), false, false); return fmaxf(as_f(r[0]), as_f(r[1])); }
DI float max32(float x) { auto r = __builtin_amdgcn_permlane32_swap(as_u(x), as_u(x), false, false); return fmaxf(as_f(r[0]), as_f(r[1])); }
DI float wave_sum(float x) { x += ror<8>(x); x += ror<4>(x); x += ror<2>(x); x += ror<1>(x); return add32(add16(x)); }

DI f32x4 mfma16(v4u a, v4u b, f32x4 c) { return __builtin_amdgcn_mfma_f32_16x16x32_bf16(__builtin_bit_cast(bf16x8, a), __builtin_bit_cast(bf16x8, b), c, 0, 0, 0); }
DI f32x16 mfma32(v4u a, v4u b, f32x16 c) { return __builtin_amdgcn_mfma_f32_32x32x16_bf16(__builtin_bit_cast(bf16x8, a), __builtin_bit_cast(bf16x8, b), c, 0, 0, 0); }

typedef LAS char* lds_t;
DI v4u lds128(lds_t L, u32 a) { return *(LAS v4u*)(L + a); }
DI v2u lds_tr(lds_t L, u32 a) { return __builtin_bit_cast(v2u, __builtin_amdgcn_ds_read_tr16_b64_v4i16((LAS s16x4*)(L + a))); }
DI v4u cat(v2u a, v2u b) { return v4u{a.x, a.y, b.x, b.y}; }

DI u32 swz(u32 row) { return ((row & 3) << 2) | ((row >> 2) & 3); }

template <int RB> DI v4u frag_tr(lds_t L, u32 base, u32 lane, u32 ks, u32 c0) {
    const u32 g = lane >> 4, q = (lane >> 2) & 3, p = lane & 3;
    const u32 r0 = 32 * ks + 8 * g + q, r1 = r0 + 4, ch = c0 + (p >> 1);
    return cat(lds_tr(L, base + r0 * RB + 16 * (ch ^ swz(r0)) + 8 * (p & 1)), lds_tr(L, base + r1 * RB + 16 * (ch ^ swz(r1)) + 8 * (p & 1)));
}

DI void wait_tag(rsrc_t rs, int off, v2u tag, bool active) {
    for (int n = 0; n < SPIN_CAP; ++n) {
        fence_compiler();
        bool ok = true;
        if (active) { const v2u f = ldw64(rs, off); ok = f.x == tag.x && f.y == tag.y; }
        if (__all(ok)) break;
        __builtin_amdgcn_s_sleep(1);
    }
    fence_compiler();
}

constexpr u32 XN_STRIDE = 1088, QS_STRIDE = 320;

DI void layernorm_to_lds(lds_t L, u32 xn_off, v4u xv, u32 w, u32 lane) {
    float f[8] = {bf_lo(xv.x), bf_hi(xv.x), bf_lo(xv.y), bf_hi(xv.y), bf_lo(xv.z), bf_hi(xv.z), bf_lo(xv.w), bf_hi(xv.w)};
    float s = ((f[0] + f[1]) + (f[2] + f[3])) + ((f[4] + f[5]) + (f[6] + f[7]));
    const float mean = wave_sum(s) * (1.0f / 512.0f);
    float v = 0.f;
#pragma unroll
    for (int i = 0; i < 8; ++i) { f[i] -= mean; v += f[i] * f[i]; }
    const float rstd = 1.0f / sqrtf(wave_sum(v) * (1.0f / 512.0f) + 1e-5f);
    *(LAS v4u*)(L + xn_off + w * XN_STRIDE + lane * 16) =
        v4u{pk(f[0] * rstd, f[1] * rstd), pk(f[2] * rstd, f[3] * rstd), pk(f[4] * rstd, f[5] * rstd), pk(f[6] * rstd, f[7] * rstd)};
}

DI void dma_slab(const char* base, u32 pitch, u32 lds, u32 w, u32 lane) {
    const u32 r = lane >> 2, ch = (lane & 3) ^ (((r >> 3) & 1) << 1);
#pragma unroll
    for (int i = 0; i < 8; ++i) dma_nt(base, (128 * w + 16 * i + r) * pitch + 16 * ch, lds + (128 * w + 16 * i) * 64);
}

DI void slab_product(lds_t L, u32 slab, u32 xn_off, u32 red_off, u32 w, u32 lane) {
    const u32 n = lane & 15, g = lane >> 4, q = (lane >> 2) & 3, p = lane & 3;
    f32x4 acc[2] = {{0.f, 0.f, 0.f, 0.f}, {0.f, 0.f, 0.f, 0.f}};
#pragma unroll
    for (int k = 0; k < 4; ++k) {
        const u32 ks = 4 * w + k;
        const v4u xb = lds128(L, xn_off + (n & 3) * XN_STRIDE + 64 * ks + 16 * g);
#pragma unroll
        for (int dt = 0; dt < 2; ++dt) {
            const u32 a0 = slab + (32 * ks + 8 * g + q) * 64 + 32 * (dt ^ (g & 1)) + 8 * p;
            acc[dt] = mfma16(cat(lds_tr(L, a0), lds_tr(L, a0 + 256)), xb, acc[dt]);
        }
    }
    if (n < 4) {
#pragma unroll
        for (int dt = 0; dt < 2; ++dt) *(LAS f32x4*)(L + red_off + (w * 4 + n) * 128 + 64 * dt + 16 * g) = acc[dt];
    }
}

DI void store_row_partials(rsrc_t rs, int region, int slots, int slot, const f32x4 (&acc)[8], u32 w, u32 lane) {
    const u32 n = lane & 15, g = lane >> 4, b = n & 3, k = n >> 2;
#pragma unroll
    for (int u = 0; u < 2; ++u) {
        const f32x4 v = sel4(k & 2, sel4(k & 1, acc[4 * u + 3], acc[4 * u + 2]), sel4(k & 1, acc[4 * u + 1], acc[4 * u]));
        const int f = 16 * b + 4 * w + 2 * u + (k >> 1);
        stw128(rs, region + (f * slots + slot) * 128 + 64 * (k & 1) + 16 * g, __builtin_bit_cast(v4u, v));
    }
}

DI void role_w(const Params& p, lds_t L, rsrc_t rs, u32 wi, u32 xcd) {
    STAMP_DECL;
    STAMP(0);
    const u32 tid = threadIdx.x, lane = tid & 63, w = __builtin_amdgcn_readfirstlane(tid >> 6);
    const u32 h = wi >> 2, j = wi & 3, n = lane & 15, g = lane >> 4;
    constexpr u32 L_WQ = 0, L_WUK = 32768, L_JUNK = 65536, L_WO = 0, L_HI = 131072;
    constexpr u32 L_XN = L_HI, L_RED = L_HI + 8192, L_QS = L_HI + 12288, L_CC = L_HI, L_CX = L_HI + 2048;

    v2u ev = ld64_issue(p.ws, WS_EPOCH);
    v4u xv = ld128_issue(p.x, tid * 16);
    if (w == 0) stw128(rs, WS_QA + ((g * 32 + h) * 4 + j) * 256 + n * 16, v4u{SENT, SENT, SENT, SENT});
    else if (w == 1) touch(p.ws, WS_QG + wi * 1024 + lane * 16, L_JUNK);
    else if (w == 2) touch(p.ws, WS_FLAGW + (lane & 7) * 1024 + (wi >> 1) * 16, L_JUNK + 1024);
    else touch(p.ws, WS_HINT3 + wi * 128 + (lane & 7) * 16, L_JUNK + 2048);
    dma_slab(p.wq + h * 256 + j * 64, 8192, L_WQ, w, lane);
#pragma unroll
    for (int i = 0; i < 8; ++i) {
        const u32 row = 32 * w + 4 * i + g;
        dma_nt(p.wuk + (128 * j) * 8192 + h * 256, row * 8192 + 16 * (n ^ (row & 15)), L_WUK + (32 * w + 4 * i) * 256);
    }
    asm volatile("s_waitcnt vmcnt(17)" : "+v"(ev), "+v"(xv) :: "memory");
    STAMP(1);
    const u64 e64 = (((u64)__builtin_amdgcn_readfirstlane(ev.y) << 32) | __builtin_amdgcn_readfirstlane(ev.x)) + 1;
    const v2u tag = {(u32)e64, (u32)(e64 >> 32)};
    layernorm_to_lds(L, L_XN, xv, w, lane);
    bar();
    wait_vm<8>();
    STAMP(2);
    if (w == 0 && lane < 8) stw64(rs, WS_FLAGW + lane * 1024 + wi * 8, tag);
    slab_product(L, L_WQ, L_XN, L_RED, w, lane);
    bar();
    if (lane < 32) {
        float s = 0.f;
#pragma unroll
        for (int ww = 0; ww < 4; ++ww) s += *(LAS float*)(L + L_RED + (ww * 4 + w) * 128 + lane * 4);
        stw64(rs, WS_QG + wi * 1024 + (w * 32 + lane) * 8, v2u{(pk(s, s) & 0xFFFFu) | (tag.x << 16), (u32)(e64 >> 16)});
    }
    STAMP(3);
    wait_vm<0>();
    v4u uk[2][4];
#pragma unroll
    for (int t = 0; t < 2; ++t)
#pragma unroll
        for (int ks = 0; ks < 4; ++ks) uk[t][ks] = lds128(L, L_WUK + (32 * w + 16 * t + n) * 256 + 16 * ((4 * ks + g) ^ n));
    STAMP(4);
    {
        v4u qg;
        for (int it = 0; it < SPIN_CAP; ++it) {
            fence_compiler();
            qg = ldw128(rs, WS_QG + (h * 4 + w) * 1024 + lane * 16);
            const u32 tl = tag.x & 0xFFFFu, th = (u32)(e64 >> 16);
            if (__all((qg.x >> 16) == tl && qg.y == th && (qg.z >> 16) == tl && qg.w == th)) break;
        }
        fence_compiler();
        *(LAS u32*)(L + L_QS + g * QS_STRIDE + 64 * w + 4 * n) = (qg.x & 0xFFFFu) | (qg.z << 16);
    }
    STAMP(5);
    bar();
    {
        f32x4 acc[2] = {{0.f, 0.f, 0.f, 0.f}, {0.f, 0.f, 0.f, 0.f}};
#pragma unroll
        for (int ks = 0; ks < 4; ++ks) {
            const v4u qb = lds128(L, L_QS + (n & 3) * QS_STRIDE + 64 * ks + 16 * g);
            acc[0] = mfma16(uk[0][ks], qb, acc[0]);
            acc[1] = mfma16(uk[1][ks], qb, acc[1]);
        }
        const float sc = 0.08838834764831845f * 1.4426950408889634f;
        const f32x4 v = sel4(n & 4, acc[1], acc[0]);
        if (n < 8) stw64(rs, WS_QA + (((n & 3) * 32 + h) * 4 + j) * 256 + 64 * w + 32 * (n >> 2) + 8 * g, v2u{pk(v.x * sc, v.y * sc), pk(v.z * sc, v.w * sc)});
    }
    STAMP(6);
    wait_tag(rs, WS_FLAGK + xcd * 1024 + (32 * w + (lane & 31)) * 8, tag, lane < 32);
    if (w == 0) wait_tag(rs, WS_HINT1 + wi * 128, tag, lane == 0);
    bar();
    STAMP(7);
#pragma unroll
    for (int i = 0; i < 8; ++i) {
        const u32 row = 32 * w + 4 * i + g;
        dma_nt(p.wuv + (128 * j) * 8192 + h * 256, row * 8192 + 16 * (n ^ swz(row)), L_HI + (32 * w + 4 * i) * 256);
    }
#pragma unroll
    for (int qq = 0; qq < 4; ++qq)
#pragma unroll
        for (int i = 0; i < 8; ++i) {
            const u32 row = 32 * ((j + qq) & 3) + 8 * w + i;
            dma_nt(p.wo + h * 131072, row * 1024 + 16 * (lane ^ swz(row)), L_WO + row * 1024);
        }
    wait_vm<32>();
    bar();
    v4u uv[2][4];
#pragma unroll
    for (int dt = 0; dt < 2; ++dt)
#pragma unroll
        for (int ks = 0; ks < 4; ++ks) uv[dt][ks] = frag_tr<256>(L, L_HI, lane, ks, 4 * w + 2 * dt);
    wait_vm<0>();
    bar();
    STAMP(8);
    v4u wo[8][4];
#pragma unroll
    for (int t = 0; t < 8; ++t)
#pragma unroll
        for (int ks = 0; ks < 4; ++ks) wo[t][ks] = frag_tr<1024>(L, L_WO, lane, ks, 16 * w + 2 * t);
    u32 dmask[4];
#pragma unroll
    for (int t = 0; t < 4; ++t) dmask[t] = (n >> 1) == (u32)t ? ((n & 1) ? 0xFFFF0000u : 0x0000FFFFu) : 0u;

    wait_tag(rs, WS_HINT2 + ((wi + 32 * w) & 127) * 128, tag, lane == 0);
    STAMP(9);
    __builtin_amdgcn_s_sleep(SLEEP_C);
    f32x4 cc = {0.f, 0.f, 0.f, 0.f};
    {
        v4u ml[4], oc[8], wa[8];
        for (int it = 0; it < SPIN_CAP; ++it) {
            fence_compiler();
#pragma unroll
            for (int i = 0; i < 4; ++i) ml[i] = ldw128(rs, WS_ML + wi * 1024 + w * 256 + g * 64 + 16 * i);
#pragma unroll
            for (int i = 0; i < 8; ++i) oc[i] = ldw128(rs, WS_OC + wi * 32768 + w * 8192 + i * 1024 + lane * 16);
            float mx = fmaxf(fmaxf(fmaxf(as_f(ml[0].x), as_f(ml[0].z)), fmaxf(as_f(ml[1].x), as_f(ml[1].z))),
                             fmaxf(fmaxf(as_f(ml[2].x), as_f(ml[2].z)), fmaxf(as_f(ml[3].x), as_f(ml[3].z))));
            mx = max32(max16(mx));
            float e[8], den = 0.f;
#pragma unroll
            for (int i = 0; i < 4; ++i) {
                e[2 * i] = __builtin_amdgcn_exp2f(as_f(ml[i].x) - mx);
                e[2 * i + 1] = __builtin_amdgcn_exp2f(as_f(ml[i].z) - mx);
                den += e[2 * i] * as_f(ml[i].y) + e[2 * i + 1] * as_f(ml[i].w);
            }
            const float inv = 1.0f / add32(add16(den));
#pragma unroll
            for (int i = 0; i < 8; ++i) {
                const u32 d = pk(e[i] * inv, e[i] * inv);
                wa[i] = v4u{d & dmask[0], d & dmask[1], d & dmask[2], d & dmask[3]};
            }
            u32 m = max(max(umax4(ml[0]), umax4(ml[1])), max(umax4(ml[2]), umax4(ml[3])));
#pragma unroll
            for (int i = 0; i < 8; ++i) m = max(m, umax4(oc[i]));
            if (__all(m != SENT)) break;
        }
        fence_compiler();
        STAMP(10);
        if (w == 0 && lane == 0) stw64(rs, WS_HINT3 + wi * 128, tag);
#pragma unroll
        for (int i = 0; i < 8; ++i) cc = mfma16(wa[i], oc[i], cc);
    }
    if (lane < 32) *(LAS v2u*)(L + L_CC + w * QS_STRIDE + 16 * n + 8 * g) = v2u{pk(cc.x, cc.y), pk(cc.z, cc.w)};
    bar();
    {
        f32x4 acc[2] = {{0.f, 0.f, 0.f, 0.f}, {0.f, 0.f, 0.f, 0.f}};
#pragma unroll
        for (int ks = 0; ks < 4; ++ks) {
            const v4u cb = lds128(L, L_CC + (n & 3) * QS_STRIDE + 64 * ks + 16 * g);
            acc[0] = mfma16(uv[0][ks], cb, acc[0]);
            acc[1] = mfma16(uv[1][ks], cb, acc[1]);
        }
        const f32x4 v = sel4(n & 4, acc[1], acc[0]);
        if (n < 8) *(LAS v2u*)(L + L_CX + (n & 3) * QS_STRIDE + 64 * w + 32 * (n >> 2) + 8 * g) = v2u{pk(v.x, v.y), pk(v.z, v.w)};
    }
    bar();
    {
        v4u cb[4];
#pragma unroll
        for (int ks = 0; ks < 4; ++ks) cb[ks] = lds128(L, L_CX + (n & 3) * QS_STRIDE + 64 * ks + 16 * g);
        f32x4 acc[8];
#pragma unroll
        for (int t = 0; t < 8; ++t) {
            acc[t] = f32x4{0.f, 0.f, 0.f, 0.f};
#pragma unroll
            for (int ks = 0; ks < 4; ++ks) acc[t] = mfma16(wo[t][ks], cb[ks], acc[t]);
        }
        store_row_partials(rs, WS_SP, 128, wi, acc, w, lane);
    }
    STAMP(11);
    STAMP_OUT();
}

template <int NI> DI void reduce_partials(rsrc_t rs, lds_t L, int off, u32 red_off, u32 w, u32 lane, bool last) {
    v4u a[NI];
    for (int it = 0; it < SPIN_CAP; ++it) {
        fence_compiler();
#pragma unroll
        for (int i = 0; i < NI; ++i) a[i] = ldw128(rs, off + ((i == NI - 1 && !last) ? 0 : i * 4096) + w * 1024 + lane * 16);
        u32 m = 0;
#pragma unroll
        for (int i = 0; i < NI; ++i) m = max(m, umax4(a[i]));
        if (__all(m != SENT)) break;
    }
    fence_compiler();
    f32x4 s = __builtin_bit_cast(f32x4, a[0]);
#pragma unroll
    for (int i = 1; i < NI; ++i)
        if (i < NI - 1 || last) s += __builtin_bit_cast(f32x4, a[i]);
#pragma unroll
    for (int e = 0; e < 4; ++e) s[e] = add32(add16(s[e] + ror<8>(s[e])));
    if (lane < 8) *(LAS f32x4*)(L + red_off + w * 128 + lane * 16) = s;
}

DI void role_k(const Params& p, lds_t L, rsrc_t rs, u32 ki, u32 xcd) {
    STAMP_DECL;
    STAMP(0);
    const u32 tid = threadIdx.x, lane = tid & 63, w = __builtin_amdgcn_readfirstlane(tid >> 6);
    const u32 b = ki >> 5, sp = ki & 31, n = lane & 15, g = lane >> 4;
    constexpr u32 L_C = 0, L_Q = 131072, L_P = L_Q, L_MX = L_Q + 8192, L_SL = L_Q + 8704;
    const v4u sent = {SENT, SENT, SENT, SENT};
    const int ml_off = WS_ML + b * 256 + (sp & 3) * 64 + (sp >> 2) * 8;

    v2u ev = ld64_issue(p.ws, WS_EPOCH);
#pragma unroll
    for (int i = 0; i < 8; ++i) stw128(rs, WS_OC + (32 * w + 4 * i + g) * 32768 + ki * 256 + n * 16, sent);
#pragma unroll
    for (int i = 0; i < 2; ++i) stw128(rs, WS_SP + ki * 8192 + i * 4096 + tid * 16, sent);
    stw128(rs, WS_YP + ki * 4096 + tid * 16, sent);
    if (lane < 32) stw64(rs, ml_off + (lane * 4 + w) * 1024, v2u{SENT, SENT});
    touch(p.ws, (w < 2 ? WS_HINT1 + w * 16384 + ki * 128 + (lane & 7) * 16 : WS_FLAGK + (lane & 7) * 1024 + (ki >> 1) * 16), L_Q + w * 1024);
    const char* cbase = p.ckv + (size_t)(b * 4096 + sp * 128) * 1024;
#define DMA_C(i) dma_nt(cbase, (32 * w + (i)) * 1024 + 16 * (lane ^ swz(i)), L_C + (32 * w + (i)) * 1024)
#pragma unroll
    for (int i = 0; i < 8; ++i) DMA_C(i);
    asm volatile("s_waitcnt vmcnt(8)" : "+v"(ev) :: "memory");
    bar();
    STAMP(1);
    const u64 e64 = (((u64)__builtin_amdgcn_readfirstlane(ev.y) << 32) | __builtin_amdgcn_readfirstlane(ev.x)) + 1;
    const v2u tag = {(u32)e64, (u32)(e64 >> 32)};
    if (lane < 2) stw64(rs, WS_FLAGK + (2 * w + lane) * 1024 + ki * 8, tag);
#pragma unroll
    for (int i = 8; i < 20; ++i) DMA_C(i);
    const int fw_off = WS_FLAGW + xcd * 1024 + (32 * w + (lane & 31)) * 8;
    v2u fw = ld64_issue(p.ws, fw_off);
#pragma unroll
    for (int i = 20; i < 32; ++i) DMA_C(i);
#undef DMA_C
    asm volatile("s_waitcnt vmcnt(12)" : "+v"(fw) :: "memory");
    if (!__all(fw.x == tag.x && fw.y == tag.y)) wait_tag(rs, fw_off, tag, true);
    if (w == 0 && lane == 0) stw64(rs, WS_HINT1 + ki * 128, tag);
    STAMP(2);
    wait_vm<Q_VMCNT>();
    STAMP(3);
    {
        v4u qv[8];
        for (int it = 0; it < SPIN_CAP; ++it) {
            fence_compiler();
#pragma unroll
            for (int i = 0; i < 8; ++i) qv[i] = ldw128(rs, WS_QA + (b * 32 + 8 * w + i) * 1024 + lane * 16);
            u32 m = 0;
#pragma unroll
            for (int i = 0; i < 8; ++i) m = max(m, umax4(qv[i]));
            if (__all(m != SENT)) break;
        }
        fence_compiler();
        STAMP(4);
#pragma unroll
        for (int i = 0; i < 8; ++i) *(LAS v4u*)(L + L_Q + (8 * w + i) * 1024 + 16 * (lane ^ swz(8 * w + i))) = qv[i];
    }
    wait_vm<0>();
    bar();
    STAMP(5);

    const u32 hd = lane & 31, hi = lane >> 5;
    f32x16 sa;
#pragma unroll
    for (int i = 0; i < 16; ++i) sa[i] = 0.f;
    {
        const u32 keyr = 16 * ((hd >> 2) & 1) + 4 * (hd >> 3) + (hd & 3);
        const u32 ab = L_C + (32 * w + keyr) * 1024, fa = swz(keyr), qb = L_Q + hd * 1024, fq = swz(hd);
#pragma unroll
        for (int s = 0; s < 32; ++s)
            sa = mfma32(lds128(L, ab + 16 * ((2 * s + hi) ^ fa)), lds128(L, qb + 16 * ((2 * s + hi) ^ fq)), sa);
    }
    float mloc = sa[0];
#pragma unroll
    for (int i = 1; i < 16; ++i) mloc = fmaxf(mloc, sa[i]);
    mloc = max32(mloc);
    bar();
    STAMP(6);
    if (lane < 32) *(LAS float*)(L + L_MX + w * 128 + lane * 4) = mloc;
    bar();
    const float mall = fmaxf(fmaxf(*(LAS float*)(L + L_MX + hd * 4), *(LAS float*)(L + L_MX + 128 + hd * 4)),
                             fmaxf(*(LAS float*)(L + L_MX + 256 + hd * 4), *(LAS float*)(L + L_MX + 384 + hd * 4)));
    {
        float ls = 0.f;
#pragma unroll
        for (int i = 0; i < 16; ++i) { sa[i] = __builtin_amdgcn_exp2f(sa[i] - mall); ls += sa[i]; }
#pragma unroll
        for (int c = 0; c < 2; ++c)
            *(LAS v4u*)(L + L_P + hd * 256 + 16 * ((4 * w + 2 * hi + c) ^ (hd & 15))) =
                v4u{pk(sa[8 * c], sa[8 * c + 1]), pk(sa[8 * c + 2], sa[8 * c + 3]), pk(sa[8 * c + 4], sa[8 * c + 5]), pk(sa[8 * c + 6], sa[8 * c + 7])};
        ls = add32(ls);
        if (lane < 32) *(LAS float*)(L + L_SL + w * 128 + lane * 4) = ls;
    }
    bar();
    STAMP(7);
    v4u pb[2][4];
#pragma unroll
    for (int ht = 0; ht < 2; ++ht)
#pragma unroll
        for (int ks = 0; ks < 4; ++ks) pb[ht][ks] = lds128(L, L_P + (16 * ht + n) * 256 + 16 * ((4 * ks + g) ^ n));
    if (lane < 32) {
        const float lsum = (*(LAS float*)(L + L_SL + lane * 4) + *(LAS float*)(L + L_SL + 128 + lane * 4)) +
                           (*(LAS float*)(L + L_SL + 256 + lane * 4) + *(LAS float*)(L + L_SL + 384 + lane * 4));
        stw64(rs, ml_off + (lane * 4 + w) * 1024, v2u{as_u(mall), as_u(lsum)});
    }
    if (w == 0 && lane == 0) stw64(rs, WS_HINT2 + ki * 128, tag);
#pragma unroll
    for (int u = 0; u < 4; ++u) {
        f32x4 acc[2][2];
#pragma unroll
        for (int e = 0; e < 2; ++e) {
            acc[e][0] = acc[e][1] = f32x4{0.f, 0.f, 0.f, 0.f};
#pragma unroll
            for (int ks = 0; ks < 4; ++ks) {
                const v4u a = frag_tr<1024>(L, L_C, lane, ks, 16 * w + 4 * u + 2 * e);
                acc[e][0] = mfma16(a, pb[0][ks], acc[e][0]);
                acc[e][1] = mfma16(a, pb[1][ks], acc[e][1]);
            }
        }
#pragma unroll
        for (int ht = 0; ht < 2; ++ht) {
            const auto r0 = __builtin_amdgcn_permlane16_swap(pk(acc[0][ht].x, acc[0][ht].y), pk(acc[1][ht].x, acc[1][ht].y), false, false);
            const auto r1 = __builtin_amdgcn_permlane16_swap(pk(acc[0][ht].z, acc[0][ht].w), pk(acc[1][ht].z, acc[1][ht].w), false, false);
            stw128(rs, WS_OC + ((16 * ht + n) * 4 + w) * 32768 + ki * 256 + 64 * u + 32 * (g & 1) + 16 * (g >> 1), v4u{r0[0], r1[0], r0[1], r1[1]});
        }
    }
    STAMP(8);

    const u32 f = ki >> 1;
    constexpr u32 L_W1 = 0, L_W2 = 32768, L_XN = 65536, L_RED = L_XN + 8192, L_HB = L_RED + 4096;
    if (ki & 1) {
        if (f < NFFN) {
            const v4u xv = *(const v4u*)(p.x + tid * 16);
            const float bias = lane < 32 ? bf_lo(*(const unsigned short*)(p.b1 + (32 * f + lane) * 2)) : 0.f;
            wait_tag(rs, WS_FLAGK + xcd * 1024 + (32 * w + (lane & 31)) * 8, tag, lane < 32);
            layernorm_to_lds(L, L_Q + 16384, xv, w, lane);
            bar();
            dma_slab(p.w1 + f * 64, 3584, L_W1, w, lane);
#pragma unroll
            for (int i = 0; i < 8; ++i) {
                const u32 row = 8 * w + i;
                dma_nt(p.w2 + (size_t)(32 * f) * 1024, row * 1024 + 16 * (lane ^ swz(row)), L_W2 + row * 1024);
            }
            wait_vm<8>();
            slab_product(L, L_W1, L_Q + 16384, L_RED, w, lane);
            wait_vm<0>();
            bar();
            STAMP(9);
            if (lane < 32) {
                float s = bias;
#pragma unroll
                for (int ww = 0; ww < 4; ++ww) s += *(LAS float*)(L + L_RED + (ww * 4 + w) * 128 + lane * 4);
                const float u = 0.7978845608028654f * (s + 0.044715f * s * s * s);
                const float th = 1.0f - 2.0f / (__expf(2.0f * u) + 1.0f);
                *(LAS unsigned short*)(L + L_HB + w * 64 + lane * 2) = (unsigned short)pk(0.5f * s * (1.0f + th), 0.f);
            }
            bar();
            const v4u hb = lds128(L, L_HB + (n & 3) * 64 + 16 * g);
            f32x4 acc[8];
#pragma unroll
            for (int t = 0; t < 8; ++t) acc[t] = mfma16(frag_tr<1024>(L, L_W2, lane, 0, 16 * w + 2 * t), hb, f32x4{0.f, 0.f, 0.f, 0.f});
            store_row_partials(rs, WS_YP, 64, f, acc, w, lane);
            STAMP(10);
        }
    } else {
        float xo = 0.f, bo = 0.f;
        if (tid < 32) {
            xo = bf_lo(*(const unsigned short*)(p.x + (32 * f + tid) * 2));
            bo = bf_lo(*(const unsigned short*)(p.b2 + (32 * (f & 15) + tid) * 2));
        }
        wait_tag(rs, WS_FLAGK + xcd * 1024 + (32 * w + (lane & 31)) * 8, tag, lane < 32);
        bar();
        for (int it = 0; it < SPIN_CAP; ++it) {
            fence_compiler();
            if (__builtin_amdgcn_readfirstlane(ldw32(rs, WS_YP + f * 8192 + ((f + 14 * w) % NFFN) * 128)) != SENT) break;
            __builtin_amdgcn_s_sleep(4);
        }
        reduce_partials<2>(rs, L, WS_YP + f * 8192, L_RED, w, lane, w < 3);
        bar();
        STAMP(9);
        float gate = 0.f;
        if (tid < 32) {
            const float y = bo + ((*(LAS float*)(L + L_RED + tid * 4) + *(LAS float*)(L + L_RED + 128 + tid * 4)) +
                                  (*(LAS float*)(L + L_RED + 256 + tid * 4) + *(LAS float*)(L + L_RED + 384 + tid * 4)));
            gate = y / (1.0f + __expf(-y));
        }
        wait_tag(rs, WS_HINT3 + ((ki + 32 * w) & 127) * 128, tag, lane == 0);
        STAMP(10);
        __builtin_amdgcn_s_sleep(SLEEP_E);
        reduce_partials<4>(rs, L, WS_SP + f * 16384, L_RED + 1024, w, lane, true);
        STAMP(11);
        bar();
        if (tid < 32) {
            const float s = (*(LAS float*)(L + L_RED + 1024 + tid * 4) + *(LAS float*)(L + L_RED + 1152 + tid * 4)) +
                            (*(LAS float*)(L + L_RED + 1280 + tid * 4) + *(LAS float*)(L + L_RED + 1408 + tid * 4));
            *(unsigned short*)(p.out + (32 * f + tid) * 2) = (unsigned short)pk(xo + s * gate, 0.f);
        }
        if (ki == 0 && tid == 0) stw64(rs, WS_EPOCH, tag);
        STAMP(12);
    }
    STAMP_OUT();
}

__global__ __launch_bounds__(256, 1) void mla_ffn_decode(Params p) {
    __shared__ __attribute__((aligned(16))) char smem[163840];
    lds_t L = (lds_t)smem;
    const rsrc_t rs = __builtin_amdgcn_make_buffer_rsrc(p.ws, 0, WS_END, 0x00020000);
    const u32 xcd = blockIdx.x & 7, r = blockIdx.x >> 3, idx = xcd * 16 + (r & 15);
    if (r < 16) role_w(p, L, rs, idx, xcd);
    else role_k(p, L, rs, idx, xcd);
}

__global__ __launch_bounds__(64, 1) void probe(unsigned* sink) {

    unsigned long long c0, c1; asm volatile("s_memrealtime %0\n\ts_waitcnt lgkmcnt(0)" : "=s"(c0) :: "memory");

    if (blockIdx.x == 0) for (int i = 0; i < 1000000; ++i) { asm volatile("s_memrealtime %0\n\ts_waitcnt lgkmcnt(0)" : "=s"(c1) :: "memory"); if (c1 - c0 > 2000) break; }

}
}

extern "C" void kernel_launch(void* const* d_in, const int* in_sizes, int n_in,
                              void* d_out, int out_size, void* d_ws, size_t ws_size,
                              hipStream_t stream) {
    mla::Params p;
    p.x = (const char*)d_in[0];
    p.ckv = (const char*)d_in[1];
    p.wq = (const char*)d_in[2];
    p.wuk = (const char*)d_in[3];
    p.wuv = (const char*)d_in[4];
    p.wo = (const char*)d_in[5];
    p.w1 = (const char*)d_in[7];
    p.b1 = (const char*)d_in[8];
    p.w2 = (const char*)d_in[9];
    p.b2 = (const char*)d_in[10];
    p.out = (char*)d_out;
    p.ws = (char*)d_ws;
    mla::mla_ffn_decode<<<256, 256, 0, stream>>>(p);
    mla::probe<<<1, 64, 0, stream>>>((unsigned*)d_ws + (32 << 20));
}
```

```cpp
#include <hip/hip_runtime.h>
#include <hip/hip_bf16.h>

namespace mla {

typedef unsigned u32;
typedef unsigned long long u64;
typedef u32 v2u __attribute__((ext_vector_type(2)));
typedef u32 v4u __attribute__((ext_vector_type(4)));
typedef float f32x2 __attribute__((ext_vector_type(2)));
typedef float f32x4 __attribute__((ext_vector_type(4)));
typedef float f32x16 __attribute__((ext_vector_type(16)));
typedef short s16x4 __attribute__((ext_vector_type(4)));
typedef __bf16 bf16x2 __attribute__((ext_vector_type(2)));
typedef __bf16 bf16x8 __attribute__((ext_vector_type(8)));
#define LAS __attribute__((address_space(3)))
#define DI __device__ __forceinline__

constexpr int SC1 = 16;
constexpr u32 SENT = 0xFFFFFFFFu;
constexpr u32 TAG_MIX = 0x5A3C96E1u;
constexpr int SPIN_CAP = 8192;
constexpr int NFFN = 112;
constexpr int OOB = 0x7FFFFF00;

constexpr int WS_EPOCH = 0;
constexpr int WS_FLAGW = 4096;
constexpr int WS_FLAGK = WS_FLAGW + 8192;
constexpr int WS_HINT1 = WS_FLAGK + 32768;
constexpr int WS_HINT3 = WS_HINT1 + 16384;
constexpr int WS_QA = 131072;
constexpr int WS_ML = WS_QA + 131072;
constexpr int WS_QG = 524288;
constexpr int WS_YP = 1048576;
constexpr int WS_SP = 2097152;
constexpr int WS_OC = 3145728;
constexpr int WS_END = WS_OC + 4194304;
static_assert(WS_ML + 131072 <= WS_YP, "workspace map");

#define Q_VMCNT 4
#define USE_HINT1 0
#define WO_NT 0
#define TOUCH_QG 1
#define QG_PACE 1
#define K_SLEEP 0
#define FW_AT 28
#define ROT 0
#define WS_BASE 0
#define SLEEP_E 8

struct Params {
    const char* x; const char* ckv; const char* wq; const char* wuk; const char* wuv; const char* wo;
    const char* w1; const char* b1; const char* w2; const char* b2;
    char* out; char* ws;
};

#define STAMP(i) do {} while (0)
#define STAMP_DECL do {} while (0)
#define STAMP_OUT() do {} while (0)

template <int N> DI void wait_vm() { asm volatile("s_waitcnt vmcnt(%0)" :: "i"(N) : "memory"); }
DI void bar() { asm volatile("s_waitcnt lgkmcnt(0)\n\ts_barrier" ::: "memory"); }
DI void fence_compiler() { asm volatile("" ::: "memory"); }
DI void pin() { __builtin_amdgcn_sched_barrier(0); }
DI u64 epoch_issue(const char* ws) { u64 e; asm volatile("s_load_dwordx2 %0, %1, 0x0" : "=s"(e) : "s"(ws) : "memory"); return e; }
DI u64 epoch_wait(u64 e) { asm volatile("s_waitcnt lgkmcnt(0)" : "+s"(e) :: "memory"); return e + 1; }

DI void dma_nt(const char* base, u32 voff, u32 lds) {
    asm volatile("s_mov_b32 m0, %0\n\ts_nop 0\n\tglobal_load_lds_dwordx4 %1, %2 nt" :: "s"(lds), "v"(voff), "s"(base) : "memory");
}
template <bool NT> DI void dma(const char* base, u32 voff, u32 lds) {
    if (NT) dma_nt(base, voff, lds);
    else asm volatile("s_mov_b32 m0, %0\n\ts_nop 0\n\tglobal_load_lds_dwordx4 %1, %2" :: "s"(lds), "v"(voff), "s"(base) : "memory");
}
DI void touch(const char* base, u32 voff, u32 lds) {
    asm volatile("s_mov_b32 m0, %0\n\ts_nop 0\n\tglobal_load_lds_dwordx4 %1, %2 sc1" :: "s"(lds), "v"(voff), "s"(base) : "memory");
}
DI v2u ld64_issue(const char* base, u32 voff) { v2u r; asm volatile("global_load_dwordx2 %0, %1, %2 sc1" : "=v"(r) : "v"(voff), "s"(base) : "memory"); return r; }
DI v2u ld64p_issue(const char* base, u32 voff) { v2u r; asm volatile("global_load_dwordx2 %0, %1, %2" : "=v"(r) : "v"(voff), "s"(base) : "memory"); return r; }
DI u32 ld16_issue(const char* base, u32 voff) { u32 r; asm volatile("global_load_ushort %0, %1, %2" : "=v"(r) : "v"(voff), "s"(base) : "memory"); return r; }
DI v4u ld128_nt(const char* ptr) { return __builtin_nontemporal_load((const __attribute__((address_space(1))) v4u*)ptr); }
DI v4u ld128_issue(const char* base, u32 voff) { v4u r; asm volatile("global_load_dwordx4 %0, %1, %2" : "=v"(r) : "v"(voff), "s"(base) : "memory"); return r; }

typedef __amdgpu_buffer_rsrc_t rsrc_t;
DI v4u ldw128(rsrc_t rs, int off) { return __builtin_amdgcn_raw_buffer_load_b128(rs, off, 0, SC1); }
DI v2u ldw64(rsrc_t rs, int off) { return __builtin_amdgcn_raw_buffer_load_b64(rs, off, 0, SC1); }
DI u32 ldw32(rsrc_t rs, int off) { return __builtin_amdgcn_raw_buffer_load_b32(rs, off, 0, SC1); }
DI void stw128(rsrc_t rs, int off, v4u v) { __builtin_amdgcn_raw_buffer_store_b128(v, rs, off, 0, SC1); }
DI void stw64(rsrc_t rs, int off, v2u v) { __builtin_amdgcn_raw_buffer_store_b64(v, rs, off, 0, SC1); }

DI float as_f(u32 x) { return __builtin_bit_cast(float, x); }
DI u32 as_u(float x) { return __builtin_bit_cast(u32, x); }
DI float bf_lo(u32 x) { return as_f(x << 16); }
DI float bf_hi(u32 x) { return as_f(x & 0xFFFF0000u); }
DI u32 pk(float lo, float hi) { f32x2 f = {lo, hi}; return __builtin_bit_cast(u32, __builtin_convertvector(f, bf16x2)); }
DI f32x4 sel4(bool c, f32x4 a, f32x4 b) { return f32x4{c ? a.x : b.x, c ? a.y : b.y, c ? a.z : b.z, c ? a.w : b.w}; }
DI u32 umax4(v4u a) { return max(max(a.x, a.y), max(a.z, a.w)); }

template <int N> DI float ror(float x) { return as_f((u32)__builtin_amdgcn_update_dpp(0, (int)as_u(x), 0x120 + N, 0xf, 0xf, false)); }
DI float add16(float x) { auto r = __builtin_amdgcn_permlane16_swap(as_u(x), as_u(x), false, false); return as_f(r[0]) + as_f(r[1]); }
DI float add32(float x) { auto r = __builtin_amdgcn_permlane32_swap(as_u(x), as_u(x), false, false); return as_f(r[0]) + as_f(r[1]); }
DI float max16(float x) { auto r = __builtin_amdgcn_permlane16_swap(as_u(x), as_u(x), false, false); return fmaxf(as_f(r[0]), as_f(r[1])); }
DI float max32(float x) { auto r = __builtin_amdgcn_permlane32_swap(as_u(x), as_u(x), false, false); return fmaxf(as_f(r[0]), as_f(r[1])); }
DI float wave_sum(float x) { x += ror<8>(x); x += ror<4>(x); x += ror<2>(x); x += ror<1>(x); return add32(add16(x)); }

DI f32x4 mfma16(v4u a, v4u b, f32x4 c) { return __builtin_amdgcn_mfma_f32_16x16x32_bf16(__builtin_bit_cast(bf16x8, a), __builtin_bit_cast(bf16x8, b), c, 0, 0, 0); }
DI f32x4 mfma16k16(v2u a, v2u b, f32x4 c) { return __builtin_amdgcn_mfma_f32_16x16x16bf16_1k(__builtin_bit_cast(s16x4, a), __builtin_bit_cast(s16x4, b), c, 0, 0, 0); }
DI f32x16 mfma32(v4u a, v4u b, f32x16 c) { return __builtin_amdgcn_mfma_f32_32x32x16_bf16(__builtin_bit_cast(bf16x8, a), __builtin_bit_cast(bf16x8, b), c, 0, 0, 0); }

typedef LAS char* lds_t;
DI v4u lds128(lds_t L, u32 a) { return *(LAS v4u*)(L + a); }
DI v2u lds_tr(lds_t L, u32 a) { return __builtin_bit_cast(v2u, __builtin_amdgcn_ds_read_tr16_b64_v4i16((LAS s16x4*)(L + a))); }
DI v4u cat(v2u a, v2u b) { return v4u{a.x, a.y, b.x, b.y}; }

DI u32 swz(u32 row) { return ((row & 3) << 2) | ((row >> 2) & 3); }

template <int RB> DI v4u frag_tr(lds_t L, u32 base, u32 lane, u32 ks, u32 c0) {
    const u32 g = lane >> 4, q = (lane >> 2) & 3, p = lane & 3;
    const u32 r0 = 32 * ks + 8 * g + q, r1 = r0 + 4, ch = c0 + (p >> 1);
    return cat(lds_tr(L, base + r0 * RB + 16 * (ch ^ swz(r0)) + 8 * (p & 1)), lds_tr(L, base + r1 * RB + 16 * (ch ^ swz(r1)) + 8 * (p & 1)));
}

DI void wait_tag(rsrc_t rs, int off, v2u tag, bool active) {
    const int o = active ? off : OOB;
#pragma unroll 1
    for (int n = 0; n < SPIN_CAP; ++n) {
        fence_compiler();
        const v2u f = ldw64(rs, o);
        if (__all(!active || (f.x == tag.x && f.y == tag.y))) break;
        __builtin_amdgcn_s_sleep(1);
    }
    fence_compiler();
}

template <bool TAGGED> DI bool pending(v4u a, v2u t) { return TAGGED ? (a.y != t.x || a.w != t.y) : umax4(a) == SENT; }
template <bool TAGGED, int N> DI bool all_valid(const v4u (&a)[N], v2u t) {
    bool bad = false;
#pragma unroll
    for (int i = 0; i < N; ++i) bad |= pending<TAGGED>(a[i], t);
    return !__any(bad);
}
template <int N> DI void gather_issue(rsrc_t rs, const int (&off)[N], v4u (&a)[N]) {
#pragma unroll
    for (int i = 0; i < N; ++i) a[i] = ldw128(rs, off[i]);
}
template <bool TAGGED, int N> DI void gather_retry(rsrc_t rs, const int (&off)[N], v4u (&a)[N], v2u tg) {
#pragma unroll 1
    for (int it = 0; it < SPIN_CAP; ++it) {
        fence_compiler();
        v4u t[N];
        bool bad[N];
#pragma unroll
        for (int i = 0; i < N; ++i) { bad[i] = pending<TAGGED>(a[i], tg); t[i] = ldw128(rs, bad[i] ? off[i] : OOB); }
#pragma unroll
        for (int i = 0; i < N; ++i) a[i] = v4u{bad[i] ? t[i].x : a[i].x, bad[i] ? t[i].y : a[i].y, bad[i] ? t[i].z : a[i].z, bad[i] ? t[i].w : a[i].w};
        if (all_valid<TAGGED>(a, tg)) break;
    }
    fence_compiler();
}

constexpr u32 XN_STRIDE = 1088, QS_STRIDE = 320;

DI v4u layernorm_row(v4u xv) {
    float f[8] = {bf_lo(xv.x), bf_hi(xv.x), bf_lo(xv.y), bf_hi(xv.y), bf_lo(xv.z), bf_hi(xv.z), bf_lo(xv.w), bf_hi(xv.w)};
    float s = ((f[0] + f[1]) + (f[2] + f[3])) + ((f[4] + f[5]) + (f[6] + f[7]));
    const float mean = wave_sum(s) * (1.0f / 512.0f);
    float v = 0.f;
#pragma unroll
    for (int i = 0; i < 8; ++i) { f[i] -= mean; v += f[i] * f[i]; }
    const float rstd = 1.0f / sqrtf(wave_sum(v) * (1.0f / 512.0f) + 1e-5f);
    return v4u{pk(f[0] * rstd, f[1] * rstd), pk(f[2] * rstd, f[3] * rstd), pk(f[4] * rstd, f[5] * rstd), pk(f[6] * rstd, f[7] * rstd)};
}

DI void store_row_partials(rsrc_t rs, int region, int slots, int slot, const f32x4 (&acc)[8], v2u t2, u32 w, u32 lane) {
    const u32 n = lane & 15, g = lane >> 4, b = n & 3, k = n >> 2;
#pragma unroll
    for (int u = 0; u < 2; ++u) {
        const f32x4 v = sel4(k & 2, sel4(k & 1, acc[4 * u + 3], acc[4 * u + 2]), sel4(k & 1, acc[4 * u + 1], acc[4 * u]));
        const int f = 16 * b + 4 * w + 2 * u + (k >> 1);
        stw128(rs, region + (f * slots + (slot ^ (f & 7))) * 128 + 64 * (k & 1) + 16 * g, v4u{pk(v.x, v.y), t2.x, pk(v.z, v.w), t2.y});
    }
}

DI void split_weights(const v4u (&a)[12], const u32 (&dmask)[4], v4u (&wa)[8]) {
    float mx = fmaxf(fmaxf(fmaxf(as_f(a[0].x), as_f(a[0].z)), fmaxf(as_f(a[1].x), as_f(a[1].z))),
                     fmaxf(fmaxf(as_f(a[2].x), as_f(a[2].z)), fmaxf(as_f(a[3].x), as_f(a[3].z))));
    mx = max32(max16(mx));
    float e[8], den = 0.f;
#pragma unroll
    for (int i = 0; i < 4; ++i) {
        e[2 * i] = __builtin_amdgcn_exp2f(as_f(a[i].x) - mx);
        e[2 * i + 1] = __builtin_amdgcn_exp2f(as_f(a[i].z) - mx);
        den += e[2 * i] * as_f(a[i].y) + e[2 * i + 1] * as_f(a[i].w);
    }
    const float inv = __builtin_amdgcn_rcpf(add32(add16(den)));
#pragma unroll
    for (int i = 0; i < 8; ++i) {
        const u32 d = pk(e[i] * inv, e[i] * inv);
        wa[i] = v4u{d & dmask[0], d & dmask[1], d & dmask[2], d & dmask[3]};
    }
}

DI void role_w(const Params& p, lds_t L, rsrc_t rs, u32 wi, u32 xcd) {
    STAMP_DECL;
    STAMP(0);
    const u32 tid = threadIdx.x, lane = tid & 63, w = __builtin_amdgcn_readfirstlane(tid >> 6);
    const u32 h = wi >> 2, j = wi & 3, n = lane & 15, g = lane >> 4;
    constexpr u32 L_WQ = 0, L_WUK = 32768, L_JUNK = 65536, L_WO = 0, L_HI = 131072;
    constexpr u32 L_XN = L_HI, L_QS = L_HI + 8192, L_CC = L_HI, L_CX = L_HI + 2048;
    const v4u sent = {SENT, SENT, SENT, SENT};
    const u32 slot0 = 8192 * w, src0 = (32 * w + g) * 8192;

    u64 e64 = epoch_issue(p.ws);
    v4u xv = ld128_issue(p.x, tid * 16);
#pragma unroll
    for (int i = 0; i < 8; ++i) dma_nt(p.wq + j * 1048576 + h * 256, src0 + 4 * i * 8192 + 16 * (n ^ swz(4 * i + g)), L_WQ + slot0 + 1024 * i);
    if (w == 0) stw128(rs, WS_QA + ((g * 32 + h) * 4 + j) * 256 + n * 16, sent);
    else touch(p.ws, w == 1 ? WS_FLAGW + (lane & 7) * 1024 + (wi >> 1) * 16 : WS_HINT3 + wi * 128 + (lane & 7) * 16, L_JUNK + 4096 + w * 1024);
    if (TOUCH_QG) touch(p.ws, WS_QG + wi * 4096 + tid * 16, L_JUNK + w * 1024);
#pragma unroll
    for (int i = 0; i < 8; ++i) dma_nt(p.wuk + j * 1048576 + h * 256, src0 + 4 * i * 8192 + 16 * (n ^ ((4 * i + g) & 15)), L_WUK + slot0 + 1024 * i);
    asm volatile("s_waitcnt vmcnt(%1)" : "+v"(xv) : "i"(17 + TOUCH_QG) : "memory");
    STAMP(1);
    *(LAS v4u*)(L + L_XN + w * XN_STRIDE + lane * 16) = layernorm_row(xv);
    e64 = epoch_wait(e64);
    const v2u tag = {(u32)e64, (u32)(e64 >> 32)}, t2 = {tag.x, tag.x ^ TAG_MIX};
    wait_vm<9 + TOUCH_QG>();
    bar();
    STAMP(2);
    {
        v4u xb[4], a[2][4];
#pragma unroll
        for (int ks = 0; ks < 4; ++ks) {
            xb[ks] = lds128(L, L_XN + (n & 3) * XN_STRIDE + 256 * j + 64 * ks + 16 * g);
            a[0][ks] = frag_tr<256>(L, L_WQ, lane, ks, 4 * w);
            a[1][ks] = frag_tr<256>(L, L_WQ, lane, ks, 4 * w + 2);
        }
        pin();
        f32x4 acc[2] = {{0.f, 0.f, 0.f, 0.f}, {0.f, 0.f, 0.f, 0.f}};
#pragma unroll
        for (int ks = 0; ks < 4; ++ks) { acc[0] = mfma16(a[0][ks], xb[ks], acc[0]); acc[1] = mfma16(a[1][ks], xb[ks], acc[1]); }
        const f32x4 v = sel4(n & 8, acc[1], acc[0]);
        stw128(rs, WS_QG + wi * 4096 + (n & 3) * 1024 + (32 * w + 16 * (n >> 3) + 4 * g + 2 * ((n >> 2) & 1)) * 8,
               v4u{as_u((n & 4) ? v.z : v.x), t2.x, as_u((n & 4) ? v.w : v.y), t2.y});
    }
    STAMP(3);
    wait_vm<1>();
    v4u uk[2][4];
#pragma unroll
    for (int t = 0; t < 2; ++t)
#pragma unroll
        for (int ks = 0; ks < 4; ++ks) uk[t][ks] = lds128(L, L_WUK + slot0 + (16 * t + n) * 256 + 16 * ((4 * ks + g) ^ n));
    if (QG_PACE) wait_vm<0>();
    STAMP(4);
    {
        v4u qg[4];
        int off[4];
#pragma unroll
        for (int i = 0; i < 4; ++i) off[i] = WS_QG + (h * 4 + i) * 4096 + w * 1024 + lane * 16;
        gather_issue(rs, off, qg);
        if (!all_valid<true>(qg, t2)) gather_retry<true>(rs, off, qg, t2);
        *(LAS u32*)(L + L_QS + w * QS_STRIDE + 4 * lane) =
            pk((as_f(qg[0].x) + as_f(qg[1].x)) + (as_f(qg[2].x) + as_f(qg[3].x)), (as_f(qg[0].z) + as_f(qg[1].z)) + (as_f(qg[2].z) + as_f(qg[3].z)));
    }
    bar();
    STAMP(5);
    {
        v4u qb[4];
#pragma unroll
        for (int ks = 0; ks < 4; ++ks) qb[ks] = lds128(L, L_QS + (n & 3) * QS_STRIDE + 64 * ks + 16 * g);
        pin();
        f32x4 acc[2] = {{0.f, 0.f, 0.f, 0.f}, {0.f, 0.f, 0.f, 0.f}};
#pragma unroll
        for (int ks = 0; ks < 4; ++ks) { acc[0] = mfma16(uk[0][ks], qb[ks], acc[0]); acc[1] = mfma16(uk[1][ks], qb[ks], acc[1]); }
        const float sc = 0.08838834764831845f * 1.4426950408889634f;
        const f32x4 v = sel4(n & 4, acc[1], acc[0]);
        if (n < 8) stw64(rs, WS_QA + (((n & 3) * 32 + h) * 4 + j) * 256 + 64 * w + 32 * (n >> 2) + 8 * g, v2u{pk(v.x * sc, v.y * sc), pk(v.z * sc, v.w * sc)});
    }
    if (lane < 2) stw64(rs, WS_FLAGW + (2 * w + lane) * 1024 + wi * 8, tag);
    STAMP(6);
    if (USE_HINT1 && w == 0) wait_tag(rs, WS_HINT1 + wi * 128, tag, lane == 0);
    bar();
    STAMP(7);
#pragma unroll
    for (int i = 0; i < 8; ++i) dma_nt(p.wuv + j * 1048576 + h * 256, src0 + 4 * i * 8192 + 16 * (n ^ swz(4 * i + g)), L_HI + slot0 + 1024 * i);
#pragma unroll
    for (int qq = 0; qq < 4; ++qq)
#pragma unroll
        for (int i = 0; i < 8; ++i) {
            const u32 row = 32 * ((j + qq) & 3) + 8 * w + i;
            if ((WO_NT >> qq) & 1) dma<true>(p.wo + h * 131072, row * 1024 + 16 * (lane ^ swz(row)), L_WO + row * 1024);
            else dma<false>(p.wo + h * 131072, row * 1024 + 16 * (lane ^ swz(row)), L_WO + row * 1024);
        }
    wait_vm<32>();
    bar();
    v4u uv[2][4];
#pragma unroll
    for (int dt = 0; dt < 2; ++dt)
#pragma unroll
        for (int ks = 0; ks < 4; ++ks) uv[dt][ks] = frag_tr<256>(L, L_HI, lane, ks, 4 * w + 2 * dt);
    wait_vm<0>();
    bar();
    STAMP(8);
    v4u wo[8][4];
#pragma unroll
    for (int t = 0; t < 8; ++t)
#pragma unroll
        for (int ks = 0; ks < 4; ++ks) wo[t][ks] = frag_tr<1024>(L, L_WO, lane, ks, 16 * w + 2 * t);
    u32 dmask[4];
#pragma unroll
    for (int t = 0; t < 4; ++t) dmask[t] = (n >> 1) == (u32)t ? ((n & 1) ? 0xFFFF0000u : 0x0000FFFFu) : 0u;
    int off[12];
#pragma unroll
    for (int i = 0; i < 4; ++i) off[i] = WS_ML + wi * 1024 + w * 256 + g * 64 + 16 * i;
#pragma unroll
    for (int i = 0; i < 8; ++i) off[4 + i] = WS_OC + wi * 32768 + w * 8192 + i * 1024 + lane * 16;

    wait_tag(rs, WS_FLAGK + xcd * 4096 + j * 1024 + (32 * w + lane) * 8, tag, lane < 32);
    STAMP(9);
    f32x4 cc = {0.f, 0.f, 0.f, 0.f};
    {
        v4u a[12], wa[8];
        gather_issue(rs, off, a);
        split_weights(a, dmask, wa);
        if (w == 0 && lane == 0) stw64(rs, WS_HINT3 + wi * 128, tag);
        f32x4 c2 = {0.f, 0.f, 0.f, 0.f};
#pragma unroll
        for (int i = 0; i < 4; ++i) { cc = mfma16(wa[2 * i], a[4 + 2 * i], cc); c2 = mfma16(wa[2 * i + 1], a[5 + 2 * i], c2); }
        cc += c2;
    }
    STAMP(10);
    if (lane < 32) *(LAS v2u*)(L + L_CC + w * QS_STRIDE + 16 * n + 8 * g) = v2u{pk(cc.x, cc.y), pk(cc.z, cc.w)};
    bar();
    STAMP(11);
    {
        v4u cb[4];
#pragma unroll
        for (int ks = 0; ks < 4; ++ks) cb[ks] = lds128(L, L_CC + (n & 3) * QS_STRIDE + 64 * ks + 16 * g);
        pin();
        f32x4 acc[2] = {{0.f, 0.f, 0.f, 0.f}, {0.f, 0.f, 0.f, 0.f}};
#pragma unroll
        for (int ks = 0; ks < 4; ++ks) { acc[0] = mfma16(uv[0][ks], cb[ks], acc[0]); acc[1] = mfma16(uv[1][ks], cb[ks], acc[1]); }
        const f32x4 v = sel4(n & 4, acc[1], acc[0]);
        if (n < 8) *(LAS v2u*)(L + L_CX + (n & 3) * QS_STRIDE + 64 * w + 32 * (n >> 2) + 8 * g) = v2u{pk(v.x, v.y), pk(v.z, v.w)};
    }
    bar();
    {
        v4u cb[4];
#pragma unroll
        for (int ks = 0; ks < 4; ++ks) cb[ks] = lds128(L, L_CX + (n & 3) * QS_STRIDE + 64 * ks + 16 * g);
        pin();
        f32x4 acc[8];
#pragma unroll
        for (int t = 0; t < 8; ++t) acc[t] = f32x4{0.f, 0.f, 0.f, 0.f};
#pragma unroll
        for (int u = 0; u < 2; ++u)
#pragma unroll
            for (int ks = 0; ks < 4; ++ks)
#pragma unroll
                for (int t = 4 * u; t < 4 * u + 4; ++t) acc[t] = mfma16(wo[t][ks], cb[ks], acc[t]);
        store_row_partials(rs, WS_SP, 128, wi, acc, t2, w, lane);
    }
    STAMP(12);
    STAMP_OUT();
}

DI void reduce_partials(rsrc_t rs, lds_t L, int base, u32 red_off, v2u t2, u32 w, u32 lane, bool last) {
    v4u a[4];
    int off[4];
#pragma unroll
    for (int i = 0; i < 4; ++i) off[i] = base + ((i == 3 && !last) ? 0 : i * 4096) + w * 1024 + lane * 16;
    gather_issue(rs, off, a);
    if (!all_valid<true>(a, t2)) gather_retry<true>(rs, off, a, t2);
    if (!last) a[3].x = a[3].z = 0;
    f32x4 s = {(bf_lo(a[0].x) + bf_lo(a[1].x)) + (bf_lo(a[2].x) + bf_lo(a[3].x)), (bf_hi(a[0].x) + bf_hi(a[1].x)) + (bf_hi(a[2].x) + bf_hi(a[3].x)),
               (bf_lo(a[0].z) + bf_lo(a[1].z)) + (bf_lo(a[2].z) + bf_lo(a[3].z)), (bf_hi(a[0].z) + bf_hi(a[1].z)) + (bf_hi(a[2].z) + bf_hi(a[3].z))};
#pragma unroll
    for (int e = 0; e < 4; ++e) s[e] = add32(add16(s[e] + ror<8>(s[e])));
    if (lane < 8) *(LAS f32x4*)(L + red_off + w * 128 + lane * 16) = s;
}

DI float gelu_tanh(float s) {
    const float u = 0.7978845608028654f * (s + 0.044715f * s * s * s);
    const float th = 1.0f - 2.0f / (__expf(2.0f * u) + 1.0f);
    return 0.5f * s * (1.0f + th);
}

DI void role_k(const Params& p, lds_t L, rsrc_t rs, u32 ki, u32 xcd) {
    STAMP_DECL;
    STAMP(0);
    const u32 tid = threadIdx.x, lane = tid & 63, w = __builtin_amdgcn_readfirstlane(tid >> 6);
    const u32 b = ki >> 5, sp = ki & 31, n = lane & 15, g = lane >> 4, q = (lane >> 2) & 3, pp = lane & 3;
    const u32 f = ki >> 1, fc = min(ki, (u32)(NFFN - 1));
    constexpr u32 L_C = 0, L_Q = 131072, L_P = L_Q, L_MX = L_Q + 8192, L_SL = L_MX + 128;
    const int ml_off = WS_ML + ((lane & 31) * 4 + w) * 1024 + b * 256 + ((sp ^ lane) & 3) * 64 + (sp >> 2) * 8;

    u64 e64 = epoch_issue(p.ws);
    if (K_SLEEP) __builtin_amdgcn_s_sleep(K_SLEEP);
    v4u xv = ld128_issue(p.x, tid * 16);
    v2u b1v = ld64p_issue(p.b1, 32 * fc + 8 * g);
    u32 xo = ld16_issue(p.x, (32 * f + (lane & 31)) * 2);
    u32 bo = ld16_issue(p.b2, (32 * (f & 15) + (lane & 31)) * 2);
    {
        const u32 fk = WS_FLAGK + lane * 4096 + (tid >> 6) * 1024 + (ki >> 1) * 16, h1 = WS_HINT1 + ki * 128 + (lane & 7) * 16;
        touch(p.ws, lane < 8 ? fk : h1, L_Q + w * 1024);
    }
    const char* cbase = p.ckv + (size_t)(b * 4096 + sp * 128) * 1024;
#define DMA_C(i) dma_nt(cbase, (32 * w + (i)) * 1024 + 16 * (lane ^ swz(i)), L_C + (32 * w + (i)) * 1024)
#pragma unroll
    for (int i = 0; i < FW_AT; ++i) DMA_C(i);
    const int fw_off = WS_FLAGW + xcd * 1024 + (32 * w + (lane & 31)) * 8;
    v2u fw = ld64_issue(p.ws, fw_off);
#pragma unroll
    for (int i = FW_AT; i < 32; ++i) DMA_C(i);
#undef DMA_C
    STAMP(1);
    asm volatile("s_waitcnt vmcnt(%5)" : "+v"(xv), "+v"(b1v), "+v"(xo), "+v"(bo), "+v"(fw) : "i"(32 - FW_AT) : "memory");
    e64 = epoch_wait(e64);
    const v2u tag = {(u32)e64, (u32)(e64 >> 32)}, t2 = {tag.x, tag.x ^ TAG_MIX};
    const v4u xn = layernorm_row(xv);
    if (!__all(fw.x == tag.x && fw.y == tag.y)) wait_tag(rs, fw_off, tag, true);
    if (USE_HINT1 == 1 && w == (ki & 3) && lane == 0) stw64(rs, WS_HINT1 + ki * 128, tag);
    STAMP(2);
    wait_vm<Q_VMCNT>();
    STAMP(3);
    {
        v4u qv[8];
        int off[8];
#pragma unroll
        for (int i = 0; i < 8; ++i) off[i] = WS_QA + (b * 32 + 8 * w + i) * 1024 + lane * 16;
        gather_issue(rs, off, qv);
        if (!all_valid<false>(qv, tag)) gather_retry<false>(rs, off, qv, tag);
        wait_vm<0>();
        STAMP(4);
        if (USE_HINT1 == 2 && w == (ki & 3) && lane == 0) stw64(rs, WS_HINT1 + ki * 128, tag);
#pragma unroll
        for (int i = 0; i < 8; ++i) *(LAS v4u*)(L + L_Q + (8 * w + i) * 1024 + 16 * (lane ^ swz(8 * w + i))) = qv[i];
    }
    fence_compiler();

    v4u w1r[4], w2r[4];
#pragma unroll
    for (int i = 0; i < 4; ++i) w1r[i] = ld128_nt(p.w1 + (128 * w + 32 * i + (lane >> 1)) * 3584 + 32 * fc + 16 * (lane & 1));
#pragma unroll
    for (int i = 0; i < 4; ++i) w2r[i] = ld128_nt(p.w2 + (16 * fc + 4 * i + g) * 1024 + 256 * w + 16 * n);

    const u32 hd = lane & 31, hi = lane >> 5;
    f32x16 sa;
#pragma unroll
    for (int i = 0; i < 16; ++i) sa[i] = 0.f;
    {
        const u32 keyr = 16 * ((hd >> 2) & 1) + 4 * (hd >> 3) + (hd & 3);
        const u32 ab = L_C + (32 * w + keyr) * 1024 + 16 * (hi ^ swz(keyr)), qb = L_Q + hd * 1024 + 16 * (hi ^ swz(hd));
        u32 aa[8], qa[8];
#pragma unroll
        for (int s = 0; s < 8; ++s) { aa[s] = ab ^ (32 * s); qa[s] = qb ^ (32 * s); }
        constexpr int LA = 4;
        v4u fa[LA + 1], fb[LA + 1];
#pragma unroll
        for (int s = 0; s < LA; ++s) fa[s] = lds128(L, aa[s]);
        bar();
        STAMP(5);
#pragma unroll
        for (int s = 0; s < LA; ++s) fb[s] = lds128(L, qa[s]);
#pragma unroll
        for (int s = 0; s < 32; ++s) {
            if (s + LA < 32) {
                fa[(s + LA) % (LA + 1)] = lds128(L, aa[(s + LA) & 7] + 256 * ((s + LA) >> 3));
                fb[(s + LA) % (LA + 1)] = lds128(L, qa[(s + LA) & 7] + 256 * ((s + LA) >> 3));
            }
            pin();
            sa = mfma32(fa[s % (LA + 1)], fb[s % (LA + 1)], sa);
            pin();
            if (s == 8) asm volatile("s_barrier" ::: "memory");
        }
    }
    const u32 t0 = L_C + (8 * g + q) * 1024 + 256 * w + 16 * ((pp >> 1) ^ swz(8 * g + q)) + 8 * (pp & 1);
    const u32 t1 = L_C + (8 * g + q + 4) * 1024 + 256 * w + 16 * ((pp >> 1) ^ swz(8 * g + q + 4)) + 8 * (pp & 1);
#define TFRAG(t, ks) cat(lds_tr(L, (t0 ^ (32 * (t))) + 32768 * (ks)), lds_tr(L, (t1 ^ (32 * (t))) + 32768 * (ks)))
    v4u ta[2][4];
#pragma unroll
    for (int ks = 0; ks < 4; ++ks) ta[0][ks] = TFRAG(0, ks);
    float mloc = sa[0];
#pragma unroll
    for (int i = 1; i < 16; ++i) mloc = fmaxf(mloc, sa[i]);
    mloc = max32(mloc);
    if (lane < 32) *(LAS float*)(L + L_MX + w * 1024 + lane * 4) = mloc;
    bar();
    STAMP(6);
    const float mall = fmaxf(fmaxf(*(LAS float*)(L + L_MX + hd * 4), *(LAS float*)(L + L_MX + 1024 + hd * 4)),
                             fmaxf(*(LAS float*)(L + L_MX + 2048 + hd * 4), *(LAS float*)(L + L_MX + 3072 + hd * 4)));
    {
        float ls = 0.f;
#pragma unroll
        for (int i = 0; i < 16; ++i) { sa[i] = __builtin_amdgcn_exp2f(sa[i] - mall); ls += sa[i]; }
#pragma unroll
        for (int c = 0; c < 2; ++c)
            *(LAS v4u*)(L + L_P + hd * 256 + 16 * ((4 * w + 2 * hi + c) ^ (hd & 15))) =
                v4u{pk(sa[8 * c], sa[8 * c + 1]), pk(sa[8 * c + 2], sa[8 * c + 3]), pk(sa[8 * c + 4], sa[8 * c + 5]), pk(sa[8 * c + 6], sa[8 * c + 7])};
        ls = add32(ls);
        if (lane < 32) *(LAS float*)(L + L_SL + w * 1024 + lane * 4) = ls;
    }
    bar();
    STAMP(7);

    v4u pb[2][4];
#pragma unroll
    for (int ks = 0; ks < 4; ++ks)
#pragma unroll
        for (int ht = 0; ht < 2; ++ht) pb[ht][ks] = lds128(L, L_P + (16 * ht + n) * 256 + 16 * ((4 * ks + g) ^ n));
    const float lsum = (*(LAS float*)(L + L_SL + hd * 4) + *(LAS float*)(L + L_SL + 1024 + hd * 4)) +
                       (*(LAS float*)(L + L_SL + 2048 + hd * 4) + *(LAS float*)(L + L_SL + 3072 + hd * 4));
    {
        f32x4 acc[3][2];
        u32 lo[2][2];
        v4u oc[4][2];
        const int oc_off = WS_OC + (n * 4 + w) * 32768 + b * 8192 + (sp ^ (n & 3)) * 256 + 32 * (g & 1) + 16 * (g >> 1);
#pragma unroll
        for (int t = 0; t < 9; ++t) {
            if (t < 7) {
#pragma unroll
                for (int ks = 0; ks < 4; ++ks) ta[(t + 1) & 1][ks] = TFRAG(t + 1, ks);
            }
            pin();
            if (t < 8) {
                acc[t % 3][0] = acc[t % 3][1] = f32x4{0.f, 0.f, 0.f, 0.f};
#pragma unroll
                for (int ks = 0; ks < 4; ++ks) {
                    acc[t % 3][0] = mfma16(ta[t & 1][ks], pb[0][ks], acc[t % 3][0]);
                    acc[t % 3][1] = mfma16(ta[t & 1][ks], pb[1][ks], acc[t % 3][1]);
                }
            }
            if (t > 0) {
                const f32x4 (&c)[2] = acc[(t - 1) % 3];
#pragma unroll
                for (int ht = 0; ht < 2; ++ht) {
                    const u32 x = pk(c[ht].x, c[ht].y), y = pk(c[ht].z, c[ht].w);
                    if (t & 1) { lo[ht][0] = x; lo[ht][1] = y; }
                    else {
                        const auto r0 = __builtin_amdgcn_permlane16_swap(lo[ht][0], x, false, false);
                        const auto r1 = __builtin_amdgcn_permlane16_swap(lo[ht][1], y, false, false);
                        oc[(t - 2) >> 1][ht] = v4u{r0[0], r1[0], r0[1], r1[1]};
                    }
                }
            }
            if (t == 0 && lane < 32) stw64(rs, ml_off, v2u{as_u(mall), as_u(lsum)});
            if (t > 0 && !(t & 1)) {
#pragma unroll
                for (int ht = 0; ht < 2; ++ht) stw128(rs, oc_off + ht * 64 * 32768 + 64 * ((t - 2) >> 1), oc[(t - 2) >> 1][ht]);
            }
            pin();
        }
    }
#undef TFRAG
    STAMP(8);
    wait_vm<0>();
    if (lane < 8) stw64(rs, WS_FLAGK + lane * 4096 + w * 1024 + ki * 8, tag);
    STAMP(15);

    constexpr u32 L_W1 = 0, L_W2 = 16384, L_XN = 32768, L_RED = L_XN + 8192;
    bar();
    if (ki < NFFN) {
        *(LAS v4u*)(L + L_XN + w * XN_STRIDE + lane * 16) = xn;
#pragma unroll
        for (int i = 0; i < 4; ++i) {
            const u32 r = lane >> 1;
            *(LAS v4u*)(L + L_W1 + (128 * w + 32 * i + (r ^ ((r >> 1) & 4))) * 32 + 16 * (lane & 1)) = w1r[i];
            *(LAS v4u*)(L + L_W2 + w * 4096 + (4 * i + g) * 256 + 16 * (n ^ (((4 * i + g) & 7) << 1))) = w2r[i];
        }
        bar();
        STAMP(9);
        f32x4 acc = {0.f, 0.f, 0.f, 0.f};
#pragma unroll
        for (int k = 0; k < 4; ++k) {
            const u32 a0 = L_W1 + (128 * w + 32 * k + 8 * g + (q ^ ((g & 1) << 2))) * 32 + 8 * pp;
            acc = mfma16(cat(lds_tr(L, a0), lds_tr(L, a0 ^ 128)), lds128(L, L_XN + (n & 3) * XN_STRIDE + 64 * (4 * w + k) + 16 * g), acc);
        }
        if (n < 4) *(LAS f32x4*)(L + L_RED + (w * 4 + n) * 64 + 16 * g) = acc;
        bar();
        f32x4 s = {bf_lo(b1v.x), bf_hi(b1v.x), bf_lo(b1v.y), bf_hi(b1v.y)};
#pragma unroll
        for (int ww = 0; ww < 4; ++ww) s += *(LAS f32x4*)(L + L_RED + (ww * 4 + (n & 3)) * 64 + 16 * g);
        const v2u hb = {pk(gelu_tanh(s.x), gelu_tanh(s.y)), pk(gelu_tanh(s.z), gelu_tanh(s.w))};
        f32x4 y[8];
#pragma unroll
        for (int t = 0; t < 8; ++t)
            y[t] = mfma16k16(lds_tr(L, L_W2 + w * 4096 + (4 * g + q) * 256 + 16 * ((2 * t + (pp >> 1)) ^ (((4 * g + q) & 7) << 1)) + 8 * (pp & 1)), hb, f32x4{0.f, 0.f, 0.f, 0.f});
        store_row_partials(rs, WS_YP, NFFN, ki, y, t2, w, lane);
        STAMP(10);
    }
    if (!(ki & 1)) {
#pragma unroll 1
        for (int it = 0; it < SPIN_CAP; ++it) {
            fence_compiler();
            if (__builtin_amdgcn_readfirstlane(ldw32(rs, WS_YP + f * (NFFN * 128) + ((f + 28 * w) % NFFN) * 128 + 4)) == t2.x) break;
            __builtin_amdgcn_s_sleep(4);
        }
        reduce_partials(rs, L, WS_YP + f * (NFFN * 128), L_RED + 1024, t2, w, lane, w < 2);
        bar();
        STAMP(11);
        float gate = 0.f;
        if (tid < 32) {
            const float y = bf_lo(bo) + ((*(LAS float*)(L + L_RED + 1024 + tid * 4) + *(LAS float*)(L + L_RED + 1152 + tid * 4)) +
                                         (*(LAS float*)(L + L_RED + 1280 + tid * 4) + *(LAS float*)(L + L_RED + 1408 + tid * 4)));
            gate = y / (1.0f + __expf(-y));
        }
        wait_tag(rs, WS_HINT3 + ((ki + 32 * w) & 127) * 128, tag, lane == 0);
        STAMP(12);
        if (SLEEP_E) __builtin_amdgcn_s_sleep(SLEEP_E);
        reduce_partials(rs, L, WS_SP + f * 16384, L_RED + 2048, t2, w, lane, true);
        bar();
        STAMP(13);
        if (tid < 32) {
            const float s = (*(LAS float*)(L + L_RED + 2048 + tid * 4) + *(LAS float*)(L + L_RED + 2176 + tid * 4)) +
                            (*(LAS float*)(L + L_RED + 2304 + tid * 4) + *(LAS float*)(L + L_RED + 2432 + tid * 4));
            *(__attribute__((address_space(1))) unsigned short*)(p.out + (32 * f + tid) * 2) = (unsigned short)pk(bf_lo(xo) + s * gate, 0.f);
        }
        if (ki == 0 && tid == 0) stw64(rs, WS_EPOCH, tag);
        STAMP(14);
    }
    STAMP_OUT();
}

__global__ __launch_bounds__(256, 1) void mla_ffn_decode(Params pa) {
    Params p = pa;
    asm volatile("" : "+s"(p.x), "+s"(p.ckv), "+s"(p.wq), "+s"(p.wuk), "+s"(p.wuv), "+s"(p.wo));
    asm volatile("" : "+s"(p.w1), "+s"(p.b1), "+s"(p.w2), "+s"(p.b2), "+s"(p.out), "+s"(p.ws));
    __shared__ __attribute__((aligned(16))) char smem[163840];
    lds_t L = (lds_t)smem;
    const rsrc_t rs = __builtin_amdgcn_make_buffer_rsrc(p.ws, 0, WS_END, 0x00020000);
    const u32 xcd = blockIdx.x & 7, r = blockIdx.x >> 3, idx = xcd * 16 + ((r + ROT) & 15);
    if (r < 16) role_w(p, L, rs, idx, xcd);
    else role_k(p, L, rs, idx, xcd);
}

}

extern "C" void kernel_launch(void* const* d_in, const int* in_sizes, int n_in,
                              void* d_out, int out_size, void* d_ws, size_t ws_size,
                              hipStream_t stream) {
    mla::Params p;
    p.x = (const char*)d_in[0];
    p.ckv = (const char*)d_in[1];
    p.wq = (const char*)d_in[2];
    p.wuk = (const char*)d_in[3];
    p.wuv = (const char*)d_in[4];
    p.wo = (const char*)d_in[5];
    p.w1 = (const char*)d_in[7];
    p.b1 = (const char*)d_in[8];
    p.w2 = (const char*)d_in[9];
    p.b2 = (const char*)d_in[10];
    p.out = (char*)d_out;
    p.ws = (char*)d_ws + WS_BASE;
    mla::mla_ffn_decode<<<256, 256, 0, stream>>>(p);
}
```

```cpp
#include <hip/hip_runtime.h>
#include <hip/hip_bf16.h>

namespace mla {

typedef unsigned u32;
typedef unsigned long long u64;
typedef u32 v2u __attribute__((ext_vector_type(2)));
typedef u32 v4u __attribute__((ext_vector_type(4)));
typedef float f32x2 __attribute__((ext_vector_type(2)));
typedef float f32x4 __attribute__((ext_vector_type(4)));
typedef float f32x16 __attribute__((ext_vector_type(16)));
typedef short s16x4 __attribute__((ext_vector_type(4)));
typedef __bf16 bf16x2 __attribute__((ext_vector_type(2)));
typedef __bf16 bf16x8 __attribute__((ext_vector_type(8)));
#define LAS __attribute__((address_space(3)))
#define DI __device__ __forceinline__

constexpr int SC1 = 16;
constexpr u32 SENT = 0xFFFFFFFFu;
constexpr u32 TAG_MIX = 0x5A3C96E1u;
constexpr int SPIN_CAP = 8192;
constexpr int NFFN = 112;
constexpr int OOB = 0x7FFFFF00;
constexpr int YP_SPAN = 64 * NFFN * 128;

constexpr int WS_EPOCH = 0;
constexpr int WS_FLAGW = 4096;
constexpr int WS_FLAGK = WS_FLAGW + 8192;
constexpr int WS_HINT1 = WS_FLAGK + 32768;
constexpr int WS_HINT3 = WS_HINT1 + 16384;
constexpr int WS_QA = 131072;
constexpr int WS_ML = WS_QA + 131072;
constexpr int WS_QG = 524288;
constexpr int WS_YP = 1048576;
constexpr int WS_SP = 2097152;
constexpr int WS_CX = WS_SP + 524288;
constexpr int WS_OC = 3145728;
constexpr int WS_END = WS_OC + 4194304;
static_assert(WS_ML + 131072 <= WS_YP, "workspace map");

#define Q_VMCNT 4
#define USE_HINT1 0
#define WO_NT 0
#define TOUCH_QG 1
#define QG_PACE 1
#define K_SLEEP 48
#define FW_AT 28
#define ROT 0
#define WS_BASE 0
#define CODE_PF 1
#define W_SLEEP 24
#define TOUCH_CX 1
#define CX_PACE 1
#define PV_NOSTORE 0
#define SLEEP_E 32

struct Params {
    const char* x; const char* ckv; const char* wq; const char* wuk; const char* wuv; const char* wo;
    const char* w1; const char* b1; const char* w2; const char* b2;
    char* out; char* ws;
};

#define STAMP(i) do {} while (0)
#define STAMP_DECL do {} while (0)
#define STAMP_OUT() do {} while (0)

template <int N> DI void wait_vm() { asm volatile("s_waitcnt vmcnt(%0)" :: "i"(N) : "memory"); }
DI void bar() { asm volatile("s_waitcnt lgkmcnt(0)\n\ts_barrier" ::: "memory"); }
DI void fence_compiler() { asm volatile("" ::: "memory"); }
DI void pin() { __builtin_amdgcn_sched_barrier(0); }
DI u64 epoch_issue(const char* ws) { u64 e; asm volatile("s_load_dwordx2 %0, %1, 0x0" : "=s"(e) : "s"(ws) : "memory"); return e; }
DI u64 epoch_wait(u64 e) { asm volatile("s_waitcnt lgkmcnt(0)" : "+s"(e) :: "memory"); return e + 1; }

DI void dma_nt(const char* base, u32 voff, u32 lds) {
    asm volatile("s_mov_b32 m0, %0\n\ts_nop 0\n\tglobal_load_lds_dwordx4 %1, %2 nt" :: "s"(lds), "v"(voff), "s"(base) : "memory");
}
template <bool NT> DI void dma(const char* base, u32 voff, u32 lds) {
    if (NT) dma_nt(base, voff, lds);
    else asm volatile("s_mov_b32 m0, %0\n\ts_nop 0\n\tglobal_load_lds_dwordx4 %1, %2" :: "s"(lds), "v"(voff), "s"(base) : "memory");
}
DI void touch(const char* base, u32 voff, u32 lds) {
    asm volatile("s_mov_b32 m0, %0\n\ts_nop 0\n\tglobal_load_lds_dwordx4 %1, %2 sc1" :: "s"(lds), "v"(voff), "s"(base) : "memory");
}
DI v2u ld64_issue(const char* base, u32 voff) { v2u r; asm volatile("global_load_dwordx2 %0, %1, %2 sc1" : "=v"(r) : "v"(voff), "s"(base) : "memory"); return r; }
DI v2u ld64p_issue(const char* base, u32 voff) { v2u r; asm volatile("global_load_dwordx2 %0, %1, %2" : "=v"(r) : "v"(voff), "s"(base) : "memory"); return r; }
DI u32 ld16_issue(const char* base, u32 voff) { u32 r; asm volatile("global_load_ushort %0, %1, %2" : "=v"(r) : "v"(voff), "s"(base) : "memory"); return r; }
DI v4u ld128_nt(const char* ptr) { return __builtin_nontemporal_load((const __attribute__((address_space(1))) v4u*)ptr); }
DI v4u ld128_issue(const char* base, u32 voff) { v4u r; asm volatile("global_load_dwordx4 %0, %1, %2" : "=v"(r) : "v"(voff), "s"(base) : "memory"); return r; }

typedef __amdgpu_buffer_rsrc_t rsrc_t;
DI v4u ldw128(rsrc_t rs, int off) { return __builtin_amdgcn_raw_buffer_load_b128(rs, off, 0, SC1); }
DI v2u ldw64(rsrc_t rs, int off) { return __builtin_amdgcn_raw_buffer_load_b64(rs, off, 0, SC1); }
DI u32 ldw32(rsrc_t rs, int off) { return __builtin_amdgcn_raw_buffer_load_b32(rs, off, 0, SC1); }
DI void stw128(rsrc_t rs, int off, v4u v) { __builtin_amdgcn_raw_buffer_store_b128(v, rs, off, 0, SC1); }
DI void stw64(rsrc_t rs, int off, v2u v) { __builtin_amdgcn_raw_buffer_store_b64(v, rs, off, 0, SC1); }

DI float as_f(u32 x) { return __builtin_bit_cast(float, x); }
DI u32 as_u(float x) { return __builtin_bit_cast(u32, x); }
DI float bf_lo(u32 x) { return as_f(x << 16); }
DI float bf_hi(u32 x) { return as_f(x & 0xFFFF0000u); }
DI u32 pk(float lo, float hi) { f32x2 f = {lo, hi}; return __builtin_bit_cast(u32, __builtin_convertvector(f, bf16x2)); }
DI f32x4 sel4(bool c, f32x4 a, f32x4 b) { return f32x4{c ? a.x : b.x, c ? a.y : b.y, c ? a.z : b.z, c ? a.w : b.w}; }
DI u32 umax4(v4u a) { return max(max(a.x, a.y), max(a.z, a.w)); }

template <int N> DI float ror(float x) { return as_f((u32)__builtin_amdgcn_update_dpp(0, (int)as_u(x), 0x120 + N, 0xf, 0xf, false)); }
DI float add16(float x) { auto r = __builtin_amdgcn_permlane16_swap(as_u(x), as_u(x), false, false); return as_f(r[0]) + as_f(r[1]); }
DI float add32(float x) { auto r = __builtin_amdgcn_permlane32_swap(as_u(x), as_u(x), false, false); return as_f(r[0]) + as_f(r[1]); }
DI float max16(float x) { auto r = __builtin_amdgcn_permlane16_swap(as_u(x), as_u(x), false, false); return fmaxf(as_f(r[0]), as_f(r[1])); }
DI float max32(float x) { auto r = __builtin_amdgcn_permlane32_swap(as_u(x), as_u(x), false, false); return fmaxf(as_f(r[0]), as_f(r[1])); }
DI float wave_sum(float x) { x += ror<8>(x); x += ror<4>(x); x += ror<2>(x); x += ror<1>(x); return add32(add16(x)); }

DI f32x4 mfma16(v4u a, v4u b, f32x4 c) { return __builtin_amdgcn_mfma_f32_16x16x32_bf16(__builtin_bit_cast(bf16x8, a), __builtin_bit_cast(bf16x8, b), c, 0, 0, 0); }
DI f32x4 mfma16k16(v2u a, v2u b, f32x4 c) { return __builtin_amdgcn_mfma_f32_16x16x16bf16_1k(__builtin_bit_cast(s16x4, a), __builtin_bit_cast(s16x4, b), c, 0, 0, 0); }
DI f32x16 mfma32(v4u a, v4u b, f32x16 c) { return __builtin_amdgcn_mfma_f32_32x32x16_bf16(__builtin_bit_cast(bf16x8, a), __builtin_bit_cast(bf16x8, b), c, 0, 0, 0); }

DI void mfma16_first(f32x4& c, v4u a, v4u b) { asm volatile("v_mfma_f32_16x16x32_bf16 %0, %1, %2, 0" : "=&v"(c) : "v"(a), "v"(b)); }
DI void mfma16_acc(f32x4& c, v4u a, v4u b) { asm volatile("v_mfma_f32_16x16x32_bf16 %0, %1, %2, %0" : "+v"(c) : "v"(a), "v"(b)); }
DI void mfma32_first(f32x16& c, v4u a, v4u b) { asm volatile("v_mfma_f32_32x32x16_bf16 %0, %1, %2, 0" : "=&v"(c) : "v"(a), "v"(b)); }
DI void mfma32_acc(f32x16& c, v4u a, v4u b) { asm volatile("v_mfma_f32_32x32x16_bf16 %0, %1, %2, %0" : "+v"(c) : "v"(a), "v"(b)); }
template <class T> DI void keep(T& x) { asm volatile("" : "+v"(x)); }

typedef LAS char* lds_t;
DI v4u lds128(lds_t L, u32 a) { return *(LAS v4u*)(L + a); }
DI v2u lds_tr(lds_t L, u32 a) { return __builtin_bit_cast(v2u, __builtin_amdgcn_ds_read_tr16_b64_v4i16((LAS s16x4*)(L + a))); }
DI v4u cat(v2u a, v2u b) { return v4u{a.x, a.y, b.x, b.y}; }

DI u32 swz(u32 row) { return ((row & 3) << 2) | ((row >> 2) & 3); }

template <int RB> DI v4u frag_tr(lds_t L, u32 base, u32 lane, u32 ks, u32 c0) {
    const u32 g = lane >> 4, q = (lane >> 2) & 3, p = lane & 3;
    const u32 r0 = 32 * ks + 8 * g + q, r1 = r0 + 4, ch = c0 + (p >> 1);
    return cat(lds_tr(L, base + r0 * RB + 16 * (ch ^ swz(r0)) + 8 * (p & 1)), lds_tr(L, base + r1 * RB + 16 * (ch ^ swz(r1)) + 8 * (p & 1)));
}

DI void wait_tag(rsrc_t rs, int off, v2u tag, bool active) {
    const int o = active ? off : OOB;
#pragma unroll 1
    for (int n = 0; n < SPIN_CAP; ++n) {
        fence_compiler();
        const v2u f = ldw64(rs, o);
        if (__all(!active || (f.x == tag.x && f.y == tag.y))) break;
        __builtin_amdgcn_s_sleep(1);
    }
    fence_compiler();
}

template <bool TAGGED> DI bool pending(v4u a, v2u t) { return TAGGED ? (a.y != t.x || a.w != t.y) : umax4(a) == SENT; }
template <bool TAGGED, int N> DI bool all_valid(const v4u (&a)[N], v2u t) {
    bool bad = false;
#pragma unroll
    for (int i = 0; i < N; ++i) bad |= pending<TAGGED>(a[i], t);
    return !__any(bad);
}
template <int N> DI void gather_issue(rsrc_t rs, const int (&off)[N], v4u (&a)[N]) {
#pragma unroll
    for (int i = 0; i < N; ++i) a[i] = ldw128(rs, off[i]);
}
template <bool TAGGED, int N> DI void gather_retry(rsrc_t rs, const int (&off)[N], v4u (&a)[N], v2u tg) {
#pragma unroll 1
    for (int it = 0; it < SPIN_CAP; ++it) {
        fence_compiler();
        v4u t[N];
        bool bad[N];
#pragma unroll
        for (int i = 0; i < N; ++i) { bad[i] = pending<TAGGED>(a[i], tg); t[i] = ldw128(rs, bad[i] ? off[i] : OOB); }
#pragma unroll
        for (int i = 0; i < N; ++i) a[i] = v4u{bad[i] ? t[i].x : a[i].x, bad[i] ? t[i].y : a[i].y, bad[i] ? t[i].z : a[i].z, bad[i] ? t[i].w : a[i].w};
        if (all_valid<TAGGED>(a, tg)) break;
    }
    fence_compiler();
}

constexpr u32 XN_STRIDE = 1088, QS_STRIDE = 320;

DI v4u layernorm_row(v4u xv) {
    float f[8] = {bf_lo(xv.x), bf_hi(xv.x), bf_lo(xv.y), bf_hi(xv.y), bf_lo(xv.z), bf_hi(xv.z), bf_lo(xv.w), bf_hi(xv.w)};
    float s = ((f[0] + f[1]) + (f[2] + f[3])) + ((f[4] + f[5]) + (f[6] + f[7]));
    const float mean = wave_sum(s) * (1.0f / 512.0f);
    float v = 0.f;
#pragma unroll
    for (int i = 0; i < 8; ++i) { f[i] -= mean; v += f[i] * f[i]; }
    const float rstd = 1.0f / sqrtf(wave_sum(v) * (1.0f / 512.0f) + 1e-5f);
    return v4u{pk(f[0] * rstd, f[1] * rstd), pk(f[2] * rstd, f[3] * rstd), pk(f[4] * rstd, f[5] * rstd), pk(f[6] * rstd, f[7] * rstd)};
}

DI void row_partial_words(const f32x4 (&acc)[8], v2u t2, u32 lane, v4u (&o)[2]) {
    const u32 k = (lane & 15) >> 2;
#pragma unroll
    for (int u = 0; u < 2; ++u) {
        const f32x4 v = sel4(k & 2, sel4(k & 1, acc[4 * u + 3], acc[4 * u + 2]), sel4(k & 1, acc[4 * u + 1], acc[4 * u]));
        o[u] = v4u{pk(v.x, v.y), t2.x, pk(v.z, v.w), t2.y};
    }
}
DI void store_row_partials(rsrc_t rs, int region, int slots, int slot, const v4u (&o)[2], u32 w, u32 lane) {
    const u32 n = lane & 15, g = lane >> 4, b = n & 3, k = n >> 2;
#pragma unroll
    for (int u = 0; u < 2; ++u) {
        const int f = 16 * b + 4 * w + 2 * u + (k >> 1);
        stw128(rs, region + (f * slots + (slot ^ (f & 7))) * 128 + 64 * (k & 1) + 16 * g, o[u]);
    }
}

DI u32 split_weight(v2u ml) {
    float mx = as_f(ml.x);
    mx = fmaxf(mx, ror<1>(mx)); mx = fmaxf(mx, ror<2>(mx)); mx = fmaxf(mx, ror<4>(mx));
    const float e = __builtin_amdgcn_exp2f(as_f(ml.x) - max32(max16(mx)));
    float den = e * as_f(ml.y);
    den += ror<1>(den); den += ror<2>(den); den += ror<4>(den);
    const float wgt = e * __builtin_amdgcn_rcpf(add32(add16(den)));
    return pk(wgt, wgt);
}
template <int I> DI v4u diag_frag(u32 wgt, const u32 (&dmask)[4]) {
    const u32 d = (u32)__builtin_amdgcn_update_dpp(0, (int)wgt, 0x150 + I, 0xf, 0xf, false);
    return v4u{d & dmask[0], d & dmask[1], d & dmask[2], d & dmask[3]};
}

DI u32 pair_row(u32 lane) { const u32 n = lane & 15; return 16 * (n >> 3) + 4 * (lane >> 4) + 2 * ((n >> 2) & 1); }
DI v4u pair_words(const f32x4 (&acc)[2], v2u t2, u32 lane) {
    const f32x4 v = sel4(lane & 8, acc[1], acc[0]);
    return v4u{as_u((lane & 4) ? v.z : v.x), t2.x, as_u((lane & 4) ? v.w : v.y), t2.y};
}
DI void gather_head_sum(rsrc_t rs, lds_t L, int region, u32 h, u32 dst, v2u t2, u32 w, u32 lane) {
    v4u a[4];
    int off[4];
#pragma unroll
    for (int i = 0; i < 4; ++i) off[i] = region + (h * 4 + i) * 4096 + w * 1024 + lane * 16;
    gather_issue(rs, off, a);
    if (!all_valid<true>(a, t2)) gather_retry<true>(rs, off, a, t2);
    *(LAS u32*)(L + dst + w * QS_STRIDE + 4 * lane) =
        pk((as_f(a[0].x) + as_f(a[1].x)) + (as_f(a[2].x) + as_f(a[3].x)), (as_f(a[0].z) + as_f(a[1].z)) + (as_f(a[2].z) + as_f(a[3].z)));
}
DI void mul_rows(lds_t L, u32 src, const v4u (&a)[2][4], f32x4 (&acc)[2], u32 lane) {
    v4u b[4];
#pragma unroll
    for (int ks = 0; ks < 4; ++ks) b[ks] = lds128(L, src + (lane & 3) * QS_STRIDE + 64 * ks + 16 * (lane >> 4));
    pin();
    acc[0] = acc[1] = f32x4{0.f, 0.f, 0.f, 0.f};
#pragma unroll
    for (int ks = 0; ks < 4; ++ks) { acc[0] = mfma16(a[0][ks], b[ks], acc[0]); acc[1] = mfma16(a[1][ks], b[ks], acc[1]); }
}

DI float gelu_tanh(float s) {
    const float u = 0.7978845608028654f * (s + 0.044715f * s * s * s);
    const float th = 1.0f - 2.0f / (__expf(2.0f * u) + 1.0f);
    return 0.5f * s * (1.0f + th);
}

DI void role_w(const Params& p, lds_t L, rsrc_t rs, u32 wi, u32 xcd) {
    STAMP_DECL;
    STAMP(0);
    const u32 tid = threadIdx.x, lane = tid & 63, w = __builtin_amdgcn_readfirstlane(tid >> 6);
    const u32 h = wi >> 2, j = wi & 3, n = lane & 15, g = lane >> 4;
    constexpr u32 L_WQ = 0, L_WUK = 32768, L_JUNK = 65536, L_WO = L_WUK, L_HI = 131072;
    constexpr u32 L_XN = L_HI, L_QS = L_HI + 8192, L_CC = 0, L_CX = 2048;
    const v4u sent = {SENT, SENT, SENT, SENT};
    const u32 slot0 = 8192 * w, src0 = (32 * w + g) * 8192;

    u64 e64 = epoch_issue(p.ws);
    const u32 fc = min(wi, (u32)(NFFN - 1));
    v4u xv = ld128_issue(p.x, tid * 16);
    v2u b1v = ld64p_issue(p.b1, 32 * fc + 8 * g);
#pragma unroll
    for (int i = 0; i < 8; ++i) dma_nt(p.wq + j * 1048576 + h * 256, src0 + 4 * i * 8192 + 16 * (n ^ swz(4 * i + g)), L_WQ + slot0 + 1024 * i);
    if (w == 0) stw128(rs, WS_QA + ((g * 32 + h) * 4 + j) * 256 + n * 16, sent);
    else touch(p.ws, w == 1 ? WS_FLAGW + (lane & 7) * 1024 + (wi >> 1) * 16 : WS_HINT3 + wi * 128 + (lane & 7) * 16, L_JUNK + 4096 + w * 1024);
    if (TOUCH_QG) touch(p.ws, WS_QG + wi * 4096 + tid * 16, L_JUNK + w * 1024);
#pragma unroll
    for (int i = 0; i < 8; ++i) dma_nt(p.wuk + j * 1048576 + h * 256, src0 + 4 * i * 8192 + 16 * (n ^ ((4 * i + g) & 15)), L_WUK + slot0 + 1024 * i);
    asm volatile("s_waitcnt vmcnt(%2)" : "+v"(xv), "+v"(b1v) : "i"(17 + TOUCH_QG) : "memory");
    STAMP(1);
    *(LAS v4u*)(L + L_XN + w * XN_STRIDE + lane * 16) = layernorm_row(xv);
    e64 = epoch_wait(e64);
    const v2u tag = {(u32)e64, (u32)(e64 >> 32)}, t2 = {tag.x, tag.x ^ TAG_MIX};
    wait_vm<9 + TOUCH_QG>();
    bar();
    STAMP(2);
    {
        v4u xb[4], a[2][4];
#pragma unroll
        for (int ks = 0; ks < 4; ++ks) {
            xb[ks] = lds128(L, L_XN + (n & 3) * XN_STRIDE + 256 * j + 64 * ks + 16 * g);
            a[0][ks] = frag_tr<256>(L, L_WQ, lane, ks, 4 * w);
            a[1][ks] = frag_tr<256>(L, L_WQ, lane, ks, 4 * w + 2);
        }
        pin();
        f32x4 acc[2] = {{0.f, 0.f, 0.f, 0.f}, {0.f, 0.f, 0.f, 0.f}};
#pragma unroll
        for (int ks = 0; ks < 4; ++ks) { acc[0] = mfma16(a[0][ks], xb[ks], acc[0]); acc[1] = mfma16(a[1][ks], xb[ks], acc[1]); }
        stw128(rs, WS_QG + wi * 4096 + (n & 3) * 1024 + (32 * w + pair_row(lane)) * 8, pair_words(acc, t2, lane));
    }
    STAMP(3);
    wait_vm<1>();
    v4u uk[2][4];
#pragma unroll
    for (int t = 0; t < 2; ++t)
#pragma unroll
        for (int ks = 0; ks < 4; ++ks) uk[t][ks] = lds128(L, L_WUK + slot0 + (16 * t + n) * 256 + 16 * ((4 * ks + g) ^ n));
    if (QG_PACE) wait_vm<0>();
    STAMP(4);
    gather_head_sum(rs, L, WS_QG, h, L_QS, t2, w, lane);
    bar();
    STAMP(5);
    {
        f32x4 acc[2];
        mul_rows(L, L_QS, uk, acc, lane);
        const float sc = 0.08838834764831845f * 1.4426950408889634f;
        const f32x4 v = sel4(n & 4, acc[1], acc[0]);
        if (n < 8) stw64(rs, WS_QA + (((n & 3) * 32 + h) * 4 + j) * 256 + 64 * w + 32 * (n >> 2) + 8 * g, v2u{pk(v.x * sc, v.y * sc), pk(v.z * sc, v.w * sc)});
    }
    if (lane < 2) stw64(rs, WS_FLAGW + (2 * w + lane) * 1024 + wi * 8, tag);
    STAMP(6);
    v4u xf[4];
#pragma unroll
    for (int k = 0; k < 4; ++k) xf[k] = lds128(L, L_XN + (n & 3) * XN_STRIDE + 64 * (4 * w + k) + 16 * g);
    if (W_SLEEP) __builtin_amdgcn_s_sleep(W_SLEEP);
    if (USE_HINT1 != 0 && w == 0) wait_tag(rs, WS_HINT1 + wi * 128, tag, lane == 0);
    bar();
    STAMP(7);
    {
        const u32 l_w1 = 8192 * w, l_w2 = l_w1 + 4096;
        const u32 r = (lane >> 1) ^ ((lane >> 2) & 4), q = (lane >> 2) & 3, pp = lane & 3;
#pragma unroll
        for (int i = 0; i < 4; ++i) dma_nt(p.w1 + 32 * fc, (128 * w + 32 * i + r) * 3584 + 16 * (lane & 1), l_w1 + 1024 * i);
#pragma unroll
        for (int i = 0; i < 4; ++i) dma_nt(p.w2 + 16384 * fc, (4 * i + g) * 1024 + 256 * w + 16 * (n ^ (((4 * i + g) & 7) << 1)), l_w2 + 1024 * i);
#pragma unroll
        for (int i = 0; i < 8; ++i) dma_nt(p.wuv + j * 1048576 + h * 256, src0 + 4 * i * 8192 + 16 * (n ^ swz(4 * i + g)), L_HI + slot0 + 1024 * i);
        wait_vm<12>();
        STAMP(13);
        f32x4 acc = {0.f, 0.f, 0.f, 0.f};
#pragma unroll
        for (int k = 0; k < 4; ++k) {
            const u32 a0 = l_w1 + (32 * k + 8 * g + (q ^ ((g & 1) << 2))) * 32 + 8 * pp;
            acc = mfma16(cat(lds_tr(L, a0), lds_tr(L, a0 ^ 128)), xf[k], acc);
        }
        if (n < 4) *(LAS f32x4*)(L + l_w1 + n * 64 + 16 * g) = acc;
#pragma unroll
        for (int i = 0; i < 8; ++i) dma_nt(p.wo + h * 131072 + j * 256, (32 * w + 4 * i + g) * 1024 + 16 * (n ^ swz(4 * i + g)), L_WO + slot0 + 1024 * i);
        if (TOUCH_CX) {
            touch(p.ws, WS_CX + wi * 4096 + tid * 16, L_JUNK + w * 1024);
            touch(p.ws, WS_SP + (g * 16 + 4 * j + w) * 8192 + h * 256 + n * 16, L_JUNK + 4096 + w * 1024);
        }
        wait_vm<16 + 2 * TOUCH_CX>();
        bar();
        f32x4 s = {bf_lo(b1v.x), bf_hi(b1v.x), bf_lo(b1v.y), bf_hi(b1v.y)};
#pragma unroll
        for (int ww = 0; ww < 4; ++ww) s += *(LAS f32x4*)(L + 8192 * ww + (n & 3) * 64 + 16 * g);
        v2u w2f[8];
#pragma unroll
        for (int t = 0; t < 8; ++t) w2f[t] = lds_tr(L, l_w2 + (4 * g + q) * 256 + 16 * ((2 * t + (pp >> 1)) ^ (((4 * g + q) & 7) << 1)) + 8 * (pp & 1));
        pin();
        const v2u hb = {pk(gelu_tanh(s.x), gelu_tanh(s.y)), pk(gelu_tanh(s.z), gelu_tanh(s.w))};
        f32x4 y[8];
#pragma unroll
        for (int t = 0; t < 8; ++t) y[t] = mfma16k16(w2f[t], hb, f32x4{0.f, 0.f, 0.f, 0.f});
        v4u yw[2];
        row_partial_words(y, t2, lane, yw);
        store_row_partials(rs, wi < NFFN ? WS_YP : OOB - YP_SPAN, NFFN, fc, yw, w, lane);
        STAMP(14);
    }
    wait_vm<2 + 2 * TOUCH_CX>();
    bar();
    STAMP(8);
    v4u uv[2][4], wo[2][4];
#pragma unroll
    for (int t = 0; t < 2; ++t)
#pragma unroll
        for (int ks = 0; ks < 4; ++ks) {
            uv[t][ks] = frag_tr<256>(L, L_HI, lane, ks, 4 * w + 2 * t);
            wo[t][ks] = frag_tr<256>(L, L_WO, lane, ks, 4 * w + 2 * t);
        }
    int cx_off = WS_CX + wi * 4096 + (n & 3) * 1024 + (32 * w + pair_row(lane)) * 8;
    int sp_off = WS_SP + ((n & 3) * 16 + 4 * j + w) * 8192 + h * 256 + pair_row(lane) * 8;
    keep(cx_off); keep(sp_off);
    u32 dmask[4];
#pragma unroll
    for (int t = 0; t < 4; ++t) dmask[t] = (n >> 1) == (u32)t ? ((n & 1) ? 0xFFFF0000u : 0x0000FFFFu) : 0u;
    int off[8], ml_off = WS_ML + (j * 128 + w * 32 + ((8 * g + (n & 7)) ^ (h & 3))) * 256 + h * 8;
#pragma unroll
    for (int i = 0; i < 8; ++i) off[i] = WS_OC + wi * 32768 + w * 8192 + (8 * g + i) * 256 + n * 16;
    keep(ml_off); keep(off[0]);

    wait_tag(rs, WS_FLAGK + xcd * 4096 + j * 1024 + (32 * w + lane) * 8, tag, lane < 32);
    STAMP(9);
    f32x4 cc = {0.f, 0.f, 0.f, 0.f};
    {
        v4u a[8];
        const v2u ml = ldw64(rs, ml_off);
        gather_issue(rs, off, a);
        const u32 wgt = split_weight(ml);
        f32x4 c2 = {0.f, 0.f, 0.f, 0.f};
        cc = mfma16(diag_frag<0>(wgt, dmask), a[0], cc); c2 = mfma16(diag_frag<1>(wgt, dmask), a[1], c2);
        cc = mfma16(diag_frag<2>(wgt, dmask), a[2], cc); c2 = mfma16(diag_frag<3>(wgt, dmask), a[3], c2);
        cc = mfma16(diag_frag<4>(wgt, dmask), a[4], cc); c2 = mfma16(diag_frag<5>(wgt, dmask), a[5], c2);
        cc = mfma16(diag_frag<6>(wgt, dmask), a[6], cc); c2 = mfma16(diag_frag<7>(wgt, dmask), a[7], c2);
        cc += c2;
    }
    keep(cc);
    STAMP(10);
    if (lane < 32) *(LAS v2u*)(L + L_CC + w * QS_STRIDE + 16 * n + 8 * g) = v2u{pk(cc.x, cc.y), pk(cc.z, cc.w)};
    bar();
    STAMP(11);
    {
        f32x4 acc[2];
        mul_rows(L, L_CC, uv, acc, lane);
        stw128(rs, cx_off, pair_words(acc, t2, lane));
        if (w == 0 && lane == 0) stw64(rs, WS_HINT3 + wi * 128, tag);
        if (CX_PACE) wait_vm<0>();
        gather_head_sum(rs, L, WS_CX, h, L_CX, t2, w, lane);
        bar();
        STAMP(15);
        mul_rows(L, L_CX, wo, acc, lane);
        stw128(rs, sp_off, pair_words(acc, t2, lane));
    }
    STAMP(12);
    STAMP_OUT();
}

DI void reduce_partials(rsrc_t rs, lds_t L, int base, u32 red_off, v2u t2, u32 w, u32 lane, bool last) {
    v4u a[4];
    int off[4];
#pragma unroll
    for (int i = 0; i < 4; ++i) off[i] = base + ((i == 3 && !last) ? 0 : i * 4096) + w * 1024 + lane * 16;
    gather_issue(rs, off, a);
    if (!all_valid<true>(a, t2)) gather_retry<true>(rs, off, a, t2);
    if (!last) a[3].x = a[3].z = 0;
    f32x4 s = {(bf_lo(a[0].x) + bf_lo(a[1].x)) + (bf_lo(a[2].x) + bf_lo(a[3].x)), (bf_hi(a[0].x) + bf_hi(a[1].x)) + (bf_hi(a[2].x) + bf_hi(a[3].x)),
               (bf_lo(a[0].z) + bf_lo(a[1].z)) + (bf_lo(a[2].z) + bf_lo(a[3].z)), (bf_hi(a[0].z) + bf_hi(a[1].z)) + (bf_hi(a[2].z) + bf_hi(a[3].z))};
#pragma unroll
    for (int e = 0; e < 4; ++e) s[e] = add32(add16(s[e] + ror<8>(s[e])));
    if (lane < 8) *(LAS f32x4*)(L + red_off + w * 128 + lane * 16) = s;
}

DI void role_k(const Params& p, lds_t L, rsrc_t rs, u32 ki, u32 xcd) {
    STAMP_DECL;
    STAMP(0);
    const u32 tid = threadIdx.x, lane = tid & 63, w = __builtin_amdgcn_readfirstlane(tid >> 6);
    const u32 b = ki >> 5, sp = ki & 31, n = lane & 15, g = lane >> 4, q = (lane >> 2) & 3, pp = lane & 3;
    const u32 f = ki >> 1;
    constexpr u32 L_C = 0, L_Q = 131072, L_P = L_Q, L_MX = L_Q + 8192;
    int ml_off = lane < 32 ? WS_ML + (w * 128 + ki) * 256 + lane * 8 : OOB;

    u64 e64 = epoch_issue(p.ws);
    if (K_SLEEP) __builtin_amdgcn_s_sleep(K_SLEEP);
    u32 xo = ld16_issue(p.x, (32 * f + (lane & 31)) * 2);
    u32 bo = ld16_issue(p.b2, (32 * (f & 15) + (lane & 31)) * 2);
    {
        const u32 fk = WS_FLAGK + lane * 4096 + (tid >> 6) * 1024 + (ki >> 1) * 16, h1 = WS_HINT1 + ki * 128 + (lane & 7) * 16;
        touch(p.ws, lane < 8 ? fk : h1, L_Q + 8192 * w);
    }
    const char* cbase = p.ckv + (size_t)(b * 4096 + sp * 128) * 1024;
    const int fw_off = WS_FLAGW + xcd * 1024 + (32 * w + (lane & 31)) * 8;
#define DMA_C(i) dma_nt(cbase, (32 * w + (i)) * 1024 + 16 * (lane ^ swz(i)), L_C + (32 * w + (i)) * 1024)
#pragma unroll
    for (int i = 0; i < FW_AT; ++i) DMA_C(i);
    v2u fw = ld64_issue(p.ws, fw_off);
#pragma unroll
    for (int i = FW_AT; i < 32; ++i) DMA_C(i);
#undef DMA_C
    STAMP(1);
    e64 = epoch_wait(e64);
    const v2u tag = {(u32)e64, (u32)(e64 >> 32)}, t2 = {tag.x, tag.x ^ TAG_MIX};
    const u32 hd = lane & 31, hi = lane >> 5;
    u32 aa[8], qa[8], tv[8][4];
    int oc_off = WS_OC + (n * 4 + w) * 32768 + b * 8192 + (sp ^ (n & 3)) * 256 + 32 * (g & 1) + 16 * (g >> 1);
    int oc_off2 = oc_off + 64 * 32768;
    {
        const u32 keyr = 16 * ((hd >> 2) & 1) + 4 * (hd >> 3) + (hd & 3);
        const u32 ab = L_C + (32 * w + keyr) * 1024 + 16 * (hi ^ swz(keyr)), qb = L_Q + hd * 1024 + 16 * (hi ^ swz(hd));
        const u32 t0 = L_C + (8 * g + q) * 1024 + 256 * w + 16 * ((pp >> 1) ^ swz(8 * g + q)) + 8 * (pp & 1);
        const u32 t1 = L_C + (8 * g + q + 4) * 1024 + 256 * w + 16 * ((pp >> 1) ^ swz(8 * g + q + 4)) + 8 * (pp & 1);
#pragma unroll
        for (int i = 0; i < 8; ++i) {
            aa[i] = ab ^ (32 * i); qa[i] = qb ^ (32 * i);
            tv[i][0] = t0 ^ (32 * i); tv[i][1] = t1 ^ (32 * i); tv[i][2] = tv[i][0] + 65536; tv[i][3] = tv[i][1] + 65536;
            keep(aa[i]); keep(qa[i]); keep(tv[i][0]); keep(tv[i][1]); keep(tv[i][2]); keep(tv[i][3]);
        }
        keep(oc_off); keep(oc_off2); keep(ml_off);
    }
    u32 mx_rd = L_MX + hd * 4, mx_wr = mx_rd + w * 1024, p_wr[2], p_rd[4], q_wr[4];
#pragma unroll
    for (int i = 0; i < 2; ++i) { p_wr[i] = L_P + hd * 256 + 16 * ((4 * w + 2 * hi + i) ^ (hd & 15)); keep(p_wr[i]); }
#pragma unroll
    for (int i = 0; i < 4; ++i) { p_rd[i] = L_P + n * 256 + 16 * ((4 * i + g) ^ n); keep(p_rd[i]); }
#pragma unroll
    for (int i = 0; i < 4; ++i) { q_wr[i] = L_Q + (8 * w + i) * 1024 + 16 * (lane ^ swz(8 * w + i)); keep(q_wr[i]); }
    keep(mx_rd); keep(mx_wr);
    asm volatile("s_waitcnt vmcnt(%3)" : "+v"(fw), "+v"(xo), "+v"(bo) : "i"(32 - FW_AT) : "memory");
    STAMP(9);
    if (!__all(fw.x == tag.x && fw.y == tag.y)) wait_tag(rs, fw_off, tag, true);
    if (USE_HINT1 == 1 && w == (ki & 3) && lane == 0) stw64(rs, WS_HINT1 + ki * 128, tag);
    STAMP(2);
    wait_vm<Q_VMCNT>();
    STAMP(3);
    {
        v4u qv[8];
        int off[8];
#pragma unroll
        for (int i = 0; i < 8; ++i) off[i] = WS_QA + (b * 32 + 8 * w + i) * 1024 + lane * 16;
        gather_issue(rs, off, qv);
        if (!all_valid<false>(qv, tag)) gather_retry<false>(rs, off, qv, tag);
        wait_vm<0>();
        STAMP(4);
        if (USE_HINT1 == 2 && w == (ki & 3) && lane == 0) stw64(rs, WS_HINT1 + ki * 128, tag);
#pragma unroll
        for (int i = 0; i < 8; ++i) *(LAS v4u*)(L + (i < 4 ? q_wr[i] : (q_wr[i & 3] ^ 16) + 4096)) = qv[i];
    }
    fence_compiler();

    f32x16 sa;
    {
        constexpr int LA = 4;
        v4u fa[LA + 1], fb[LA + 1];
#pragma unroll
        for (int s = 0; s < LA; ++s) fa[s] = lds128(L, aa[s]);
        bar();
        STAMP(5);
#pragma unroll
        for (int s = 0; s < LA; ++s) fb[s] = lds128(L, qa[s]);
#pragma unroll
        for (int s = 0; s < 32; ++s) {
            if (s + LA < 32) {
                fa[(s + LA) % (LA + 1)] = lds128(L, aa[(s + LA) & 7] + 256 * ((s + LA) >> 3));
                fb[(s + LA) % (LA + 1)] = lds128(L, qa[(s + LA) & 7] + 256 * ((s + LA) >> 3));
            }
            pin();
            if (s == 0) mfma32_first(sa, fa[0], fb[0]);
            else mfma32_acc(sa, fa[s % (LA + 1)], fb[s % (LA + 1)]);
            pin();
            if (s == 8) asm volatile("s_barrier" ::: "memory");
        }
    }
#define THALF(t, ks, hf) lds_tr(L, tv[t][(hf) + ((ks) & 2)] + 32768 * ((ks) & 1))
    v2u ta[2][4][2];
#pragma unroll
    for (int ks = 0; ks < 4; ++ks) { ta[0][ks][0] = THALF(0, ks, 0); ta[0][ks][1] = THALF(0, ks, 1); }
    pin();
    asm volatile("s_nop 7\n\ts_nop 3" : "+v"(sa));
    float mloc = fmaxf(fmaxf(fmaxf(fmaxf(sa[0], sa[1]), sa[2]), fmaxf(fmaxf(sa[3], sa[4]), sa[5])), fmaxf(fmaxf(fmaxf(sa[6], sa[7]), sa[8]), fmaxf(fmaxf(sa[9], sa[10]), sa[11])));
    mloc = max32(fmaxf(fmaxf(fmaxf(sa[12], sa[13]), fmaxf(sa[14], sa[15])), mloc));
    *(LAS float*)(L + mx_wr) = mloc;
    bar();
    STAMP(6);
    const float mall = fmaxf(fmaxf(*(LAS float*)(L + mx_rd), *(LAS float*)(L + mx_rd + 1024)), fmaxf(*(LAS float*)(L + mx_rd + 2048), *(LAS float*)(L + mx_rd + 3072)));
    {
#pragma unroll
        for (int c = 0; c < 2; ++c) {
#pragma unroll
            for (int i = 8 * c; i < 8 * c + 8; ++i) sa[i] = __builtin_amdgcn_exp2f(sa[i] - mall);
            *(LAS v4u*)(L + p_wr[c]) = v4u{pk(sa[8 * c], sa[8 * c + 1]), pk(sa[8 * c + 2], sa[8 * c + 3]), pk(sa[8 * c + 4], sa[8 * c + 5]), pk(sa[8 * c + 6], sa[8 * c + 7])};
        }
        f32x2 l2[4];
#pragma unroll
        for (int i = 0; i < 4; ++i) l2[i] = f32x2{sa[4 * i], sa[4 * i + 1]} + f32x2{sa[4 * i + 2], sa[4 * i + 3]};
        const f32x2 l1 = (l2[0] + l2[1]) + (l2[2] + l2[3]);
        *(LAS float*)(L + mx_wr + 128) = add32(l1.x + l1.y);
    }
    bar();
    STAMP(7);

    v4u pb[2][4];
#pragma unroll
    for (int ks = 0; ks < 4; ++ks)
#pragma unroll
        for (int ht = 0; ht < 2; ++ht) pb[ht][ks] = lds128(L, p_rd[ks] + 4096 * ht);
    pin();
    float sl[4];
#pragma unroll
    for (int i = 0; i < 4; ++i) sl[i] = *(LAS float*)(L + mx_rd + 128 + 1024 * i);
    pin();
    {
        f32x4 acc[2][2];
        u32 pc[2][2][2];
        v4u oc[2];
#pragma unroll
        for (int t = 0; t < 9; ++t) {
#pragma unroll
            for (int i = 0; i < 8; ++i) {
                const int ks = i >> 1, ht = i & 1;
                if (t < 8) {
                    const v4u a = cat(ta[t & 1][ks][0], ta[t & 1][ks][1]);
                    if (ks == 0) mfma16_first(acc[t & 1][ht], a, pb[ht][0]);
                    else mfma16_acc(acc[t & 1][ht], a, pb[ht][ks]);
                } else if (i == 0) {
                    asm volatile("s_nop 7" : "+v"(acc[1][0]), "+v"(acc[1][1]));
                }
                pin();
                if (t < 7) ta[(t + 1) & 1][ks][ht] = THALF(t + 1, ks, ht);
                if (t == 1 && i == 0) stw64(rs, ml_off, v2u{as_u(mall), as_u((sl[0] + sl[1]) + (sl[2] + sl[3]))});
                if (t > 0) {
                    const int u = (t - 1) & 1;
                    if (i == 3 || i == 4) {
                        const f32x4 c = acc[u][i - 3];
                        pc[u][i - 3][0] = pk(c.x, c.y);
                        pc[u][i - 3][1] = pk(c.z, c.w);
                    }
                    if (u == 1 && (i == 5 || i == 6)) {
                        const auto r0 = __builtin_amdgcn_permlane16_swap(pc[0][i - 5][0], pc[1][i - 5][0], false, false);
                        const auto r1 = __builtin_amdgcn_permlane16_swap(pc[0][i - 5][1], pc[1][i - 5][1], false, false);
                        oc[i - 5] = v4u{r0[0], r1[0], r0[1], r1[1]};
                        if (PV_NOSTORE) { keep(oc[i - 5]); } else if (i == 5 || t == 8) stw128(rs, (i == 5 ? oc_off : oc_off2) + 64 * ((t - 2) >> 1), oc[i - 5]);
                    }
                    if (!PV_NOSTORE && u == 0 && t > 1 && i == 5) stw128(rs, oc_off2 + 64 * ((t - 3) >> 1), oc[1]);
                }
                pin();
            }
        }
    }
#undef THALF
    STAMP(8);
    wait_vm<0>();
    if (lane < 8) stw64(rs, WS_FLAGK + lane * 4096 + w * 1024 + ki * 8, tag);
    STAMP(15);

    constexpr u32 L_RED = L_Q + 16384;
    if (!(ki & 1)) {
#pragma unroll 1
        for (int it = 0; it < SPIN_CAP; ++it) {
            fence_compiler();
            if (__builtin_amdgcn_readfirstlane(ldw32(rs, WS_YP + f * (NFFN * 128) + ((f + 28 * w) % NFFN) * 128 + 4)) == t2.x) break;
            __builtin_amdgcn_s_sleep(4);
        }
        reduce_partials(rs, L, WS_YP + f * (NFFN * 128), L_RED + 1024, t2, w, lane, w < 2);
        bar();
        STAMP(11);
        float gate = 0.f;
        if (tid < 32) {
            const float y = bf_lo(bo) + ((*(LAS float*)(L + L_RED + 1024 + tid * 4) + *(LAS float*)(L + L_RED + 1152 + tid * 4)) +
                                         (*(LAS float*)(L + L_RED + 1280 + tid * 4) + *(LAS float*)(L + L_RED + 1408 + tid * 4)));
            gate = y / (1.0f + __expf(-y));
        }
        wait_tag(rs, WS_HINT3 + ((ki + 32 * w) & 127) * 128, tag, lane == 0);
        STAMP(12);
        if (SLEEP_E) __builtin_amdgcn_s_sleep(SLEEP_E);
        {
            v4u a[2];
            const int off[2] = {(int)(WS_SP + f * 8192 + tid * 16), (int)(WS_SP + f * 8192 + 4096 + tid * 16)};
            gather_issue(rs, off, a);
            if (!all_valid<true>(a, t2)) gather_retry<true>(rs, off, a, t2);
            const f32x2 s2 = {add32(add16(as_f(a[0].x) + as_f(a[1].x))), add32(add16(as_f(a[0].z) + as_f(a[1].z)))};
            if (lane < 16) *(LAS f32x2*)(L + L_RED + 2048 + w * 128 + lane * 8) = s2;
        }
        bar();
        STAMP(13);
        if (tid < 32) {
            const float s = (*(LAS float*)(L + L_RED + 2048 + tid * 4) + *(LAS float*)(L + L_RED + 2176 + tid * 4)) +
                            (*(LAS float*)(L + L_RED + 2304 + tid * 4) + *(LAS float*)(L + L_RED + 2432 + tid * 4));
            *(__attribute__((address_space(1))) unsigned short*)(p.out + (32 * f + tid) * 2) = (unsigned short)pk(bf_lo(xo) + s * gate, 0.f);
        }
        if (ki == 0 && tid == 0) stw64(rs, WS_EPOCH, tag);
        STAMP(14);
    }
    STAMP_OUT();
}

DI void code_prefetch(u32 r) {
    u64 pc;
    int len_k, len_w;
    asm volatile("s_getpc_b64 %0\n.Lmla_code_begin:\n\ts_mov_b32 %1, .Lmla_code_end_k-.Lmla_code_begin\n\ts_mov_b32 %2, .Lmla_code_end_w-.Lmla_code_begin"
                 : "=s"(pc), "=s"(len_k), "=s"(len_w));
    const int len = max(len_k, len_w);
    const u32 lane = threadIdx.x & 63, w = __builtin_amdgcn_readfirstlane(threadIdx.x >> 6);
    const int off = min((int)(r * 1024 + w * 256 + (lane & 3) * 64), max(len - 4, 0));
    asm volatile("s_mov_b32 m0, %0\n\ts_nop 0\n\tglobal_load_lds_dword %1, %2" :: "s"(131072 + 6144 + 8192 * w), "v"(off), "s"(pc) : "memory");
}

__global__ __launch_bounds__(256, 1) void mla_ffn_decode(Params pa) {
    if (CODE_PF) code_prefetch(blockIdx.x >> 3);
    Params p = pa;
    asm volatile("" : "+s"(p.x), "+s"(p.ckv), "+s"(p.wq), "+s"(p.wuk), "+s"(p.wuv), "+s"(p.wo));
    asm volatile("" : "+s"(p.w1), "+s"(p.b1), "+s"(p.w2), "+s"(p.b2), "+s"(p.out), "+s"(p.ws));
    __shared__ __attribute__((aligned(16))) char smem[163840];
    lds_t L = (lds_t)smem;
    const rsrc_t rs = __builtin_amdgcn_make_buffer_rsrc(p.ws, 0, WS_END, 0x00020000);
    const u32 xcd = blockIdx.x & 7, r = blockIdx.x >> 3, idx = xcd * 16 + ((r + ROT) & 15);
    if (r >= 16) {
        role_k(p, L, rs, idx, xcd);
        asm volatile(".Lmla_code_end_k:");
    } else {
        role_w(p, L, rs, idx, xcd);
        asm volatile(".Lmla_code_end_w:");
    }
}

}

extern "C" void kernel_launch(void* const* d_in, const int* in_sizes, int n_in,
                              void* d_out, int out_size, void* d_ws, size_t ws_size,
                              hipStream_t stream) {
    mla::Params p;
    p.x = (const char*)d_in[0];
    p.ckv = (const char*)d_in[1];
    p.wq = (const char*)d_in[2];
    p.wuk = (const char*)d_in[3];
    p.wuv = (const char*)d_in[4];
    p.wo = (const char*)d_in[5];
    p.w1 = (const char*)d_in[7];
    p.b1 = (const char*)d_in[8];
    p.w2 = (const char*)d_in[9];
    p.b2 = (const char*)d_in[10];
    p.out = (char*)d_out;
    p.ws = (char*)d_ws + WS_BASE;
    mla::mla_ffn_decode<<<256, 256, 0, stream>>>(p);
}
```

```cpp
#include <hip/hip_runtime.h>
#include <hip/hip_bf16.h>

namespace mla {

typedef unsigned u32;
typedef unsigned long long u64;
typedef u32 v2u __attribute__((ext_vector_type(2)));
typedef u32 v4u __attribute__((ext_vector_type(4)));
typedef float f32x2 __attribute__((ext_vector_type(2)));
typedef float f32x4 __attribute__((ext_vector_type(4)));
typedef float f32x16 __attribute__((ext_vector_type(16)));
typedef short s16x4 __attribute__((ext_vector_type(4)));
typedef __bf16 bf16x2 __attribute__((ext_vector_type(2)));
typedef __bf16 bf16x8 __attribute__((ext_vector_type(8)));
#define LAS __attribute__((address_space(3)))
#define DI __device__ __forceinline__

constexpr int SC1 = 16;
constexpr u32 SENT = 0xFFFFFFFFu;
constexpr u32 TAG_MIX = 0x5A3C96E1u;
constexpr int SPIN_CAP = 8192;
constexpr int NFFN = 112;
constexpr int OOB = 0x7FFFFF00;
constexpr int YP_SPAN = 64 * NFFN * 128;

constexpr int WS_EPOCH = 0;
constexpr int WS_FLAGW = 4096;
constexpr int WS_FLAGK = WS_FLAGW + 8192;
constexpr int WS_HINT1 = WS_FLAGK + 32768;
constexpr int WS_HINT3 = WS_HINT1 + 16384;
constexpr int WS_QA = 131072;
constexpr int WS_ML = WS_QA + 131072;
constexpr int WS_QG = 524288;
constexpr int WS_YP = 1048576;
constexpr int WS_SP = 2097152;
constexpr int WS_CX = WS_SP + 524288;
constexpr int WS_OC = 3145728;
constexpr int WS_END = WS_OC + 4194304;
static_assert(WS_ML + 131072 <= WS_YP, "workspace map");

#define Q_VMCNT 4
#define USE_HINT1 0
#define WO_NT 0
#define TOUCH_QG 1
#define QG_PACE 1
#define K_SLEEP 48
#define FW_AT 28
#define ROT 0
#define WS_BASE 0
#define CODE_PF 1
#define W_SLEEP 0
#define TOUCH_CX 1
#define CX_PACE 1
#define HELP_W 0
#define Q_SLEEP 0
#define PV_D 6
#define W_START 350
#define H3_EARLY 1
#define ML_NAP 2
#define FFN_MAP 1
#define HELP_K 0
#define PV_NOSTORE 0
#define SLEEP_E 32

struct Params {
    const char* x; const char* ckv; const char* wq; const char* wuk; const char* wuv; const char* wo;
    const char* w1; const char* b1; const char* w2; const char* b2;
    char* out; char* ws;
};

#define STAMP(i) do {} while (0)
#define STAMP_DECL do {} while (0)
#define STAMP_OUT() do {} while (0)

template <int N> DI void wait_vm() { asm volatile("s_waitcnt vmcnt(%0)" :: "i"(N) : "memory"); }
DI void bar() { asm volatile("s_waitcnt lgkmcnt(0)\n\ts_barrier" ::: "memory"); }
DI void fence_compiler() { asm volatile("" ::: "memory"); }
DI void pin() { __builtin_amdgcn_sched_barrier(0); }
DI u64 epoch_issue(const char* ws) { u64 e; asm volatile("s_load_dwordx2 %0, %1, 0x0" : "=s"(e) : "s"(ws) : "memory"); return e; }
DI u64 epoch_wait(u64 e) { asm volatile("s_waitcnt lgkmcnt(0)" : "+s"(e) :: "memory"); return e + 1; }

DI void dma_nt(const char* base, u32 voff, u32 lds) {
    asm volatile("s_mov_b32 m0, %0\n\ts_nop 0\n\tglobal_load_lds_dwordx4 %1, %2 nt" :: "s"(lds), "v"(voff), "s"(base) : "memory");
}
template <bool NT> DI void dma(const char* base, u32 voff, u32 lds) {
    if (NT) dma_nt(base, voff, lds);
    else asm volatile("s_mov_b32 m0, %0\n\ts_nop 0\n\tglobal_load_lds_dwordx4 %1, %2" :: "s"(lds), "v"(voff), "s"(base) : "memory");
}
DI void touch(const char* base, u32 voff, u32 lds) {
    asm volatile("s_mov_b32 m0, %0\n\ts_nop 0\n\tglobal_load_lds_dwordx4 %1, %2 sc1" :: "s"(lds), "v"(voff), "s"(base) : "memory");
}
DI void touch_word(const char* base, u32 voff, u32 lds) {
    asm volatile("s_mov_b32 m0, %0\n\ts_nop 0\n\tglobal_load_lds_dword %1, %2 nt" :: "s"(lds), "v"(voff), "s"(base) : "memory");
}
DI v2u ld64_issue(const char* base, u32 voff) { v2u r; asm volatile("global_load_dwordx2 %0, %1, %2 sc1" : "=v"(r) : "v"(voff), "s"(base) : "memory"); return r; }
DI v2u ld64p_issue(const char* base, u32 voff) { v2u r; asm volatile("global_load_dwordx2 %0, %1, %2" : "=v"(r) : "v"(voff), "s"(base) : "memory"); return r; }
DI u32 ld32_issue(const char* base, u32 voff) { u32 r; asm volatile("global_load_dword %0, %1, %2" : "=v"(r) : "v"(voff), "s"(base) : "memory"); return r; }
DI v4u ld128_nt(const char* ptr) { return __builtin_nontemporal_load((const __attribute__((address_space(1))) v4u*)ptr); }
DI v4u ld128_issue(const char* base, u32 voff) { v4u r; asm volatile("global_load_dwordx4 %0, %1, %2" : "=v"(r) : "v"(voff), "s"(base) : "memory"); return r; }

typedef __amdgpu_buffer_rsrc_t rsrc_t;
DI v4u ldw128(rsrc_t rs, int off) { return __builtin_amdgcn_raw_buffer_load_b128(rs, off, 0, SC1); }
DI v2u ldw64(rsrc_t rs, int off) { return __builtin_amdgcn_raw_buffer_load_b64(rs, off, 0, SC1); }
DI u32 ldw32(rsrc_t rs, int off) { return __builtin_amdgcn_raw_buffer_load_b32(rs, off, 0, SC1); }
DI void stw128(rsrc_t rs, int off, v4u v) { __builtin_amdgcn_raw_buffer_store_b128(v, rs, off, 0, SC1); }
DI void stw64(rsrc_t rs, int off, v2u v) { __builtin_amdgcn_raw_buffer_store_b64(v, rs, off, 0, SC1); }

DI float as_f(u32 x) { return __builtin_bit_cast(float, x); }
DI u32 as_u(float x) { return __builtin_bit_cast(u32, x); }
DI float bf_lo(u32 x) { return as_f(x << 16); }
DI float bf_hi(u32 x) { return as_f(x & 0xFFFF0000u); }
DI u32 pk(float lo, float hi) { f32x2 f = {lo, hi}; return __builtin_bit_cast(u32, __builtin_convertvector(f, bf16x2)); }
DI f32x4 sel4(bool c, f32x4 a, f32x4 b) { return f32x4{c ? a.x : b.x, c ? a.y : b.y, c ? a.z : b.z, c ? a.w : b.w}; }
DI u32 umax4(v4u a) { return max(max(a.x, a.y), max(a.z, a.w)); }

template <int N> DI float ror(float x) { return as_f((u32)__builtin_amdgcn_update_dpp(0, (int)as_u(x), 0x120 + N, 0xf, 0xf, false)); }
DI float add16(float x) { auto r = __builtin_amdgcn_permlane16_swap(as_u(x), as_u(x), false, false); return as_f(r[0]) + as_f(r[1]); }
DI float add32(float x) { auto r = __builtin_amdgcn_permlane32_swap(as_u(x), as_u(x), false, false); return as_f(r[0]) + as_f(r[1]); }
DI float max16(float x) { auto r = __builtin_amdgcn_permlane16_swap(as_u(x), as_u(x), false, false); return fmaxf(as_f(r[0]), as_f(r[1])); }
DI float max32(float x) { auto r = __builtin_amdgcn_permlane32_swap(as_u(x), as_u(x), false, false); return fmaxf(as_f(r[0]), as_f(r[1])); }
DI float wave_sum(float x) { x += ror<8>(x); x += ror<4>(x); x += ror<2>(x); x += ror<1>(x); return add32(add16(x)); }

DI f32x4 mfma16(v4u a, v4u b, f32x4 c) { return __builtin_amdgcn_mfma_f32_16x16x32_bf16(__builtin_bit_cast(bf16x8, a), __builtin_bit_cast(bf16x8, b), c, 0, 0, 0); }
DI f32x4 mfma16k16(v2u a, v2u b, f32x4 c) { return __builtin_amdgcn_mfma_f32_16x16x16bf16_1k(__builtin_bit_cast(s16x4, a), __builtin_bit_cast(s16x4, b), c, 0, 0, 0); }
DI f32x16 mfma32(v4u a, v4u b, f32x16 c) { return __builtin_amdgcn_mfma_f32_32x32x16_bf16(__builtin_bit_cast(bf16x8, a), __builtin_bit_cast(bf16x8, b), c, 0, 0, 0); }

DI void mfma16_first(f32x4& c, v4u a, v4u b) { asm volatile("v_mfma_f32_16x16x32_bf16 %0, %1, %2, 0" : "=&v"(c) : "v"(a), "v"(b)); }
DI void mfma16_acc(f32x4& c, v4u a, v4u b) { asm volatile("v_mfma_f32_16x16x32_bf16 %0, %1, %2, %0" : "+v"(c) : "v"(a), "v"(b)); }
DI void mfma32_first(f32x16& c, v4u a, v4u b) { asm volatile("v_mfma_f32_32x32x16_bf16 %0, %1, %2, 0" : "=&v"(c) : "v"(a), "v"(b)); }
DI void mfma32_acc(f32x16& c, v4u a, v4u b) { asm volatile("v_mfma_f32_32x32x16_bf16 %0, %1, %2, %0" : "+v"(c) : "v"(a), "v"(b)); }
DI void mfma_settle(f32x4& c0, f32x4& c1) { asm volatile("s_nop 7" : "+v"(c0), "+v"(c1)); }
template <class T> DI void keep(T& x) { asm volatile("" : "+v"(x)); }

typedef LAS char* lds_t;
DI v4u lds128(lds_t L, u32 a) { return *(LAS v4u*)(L + a); }
DI v2u lds_tr(lds_t L, u32 a) { return __builtin_bit_cast(v2u, __builtin_amdgcn_ds_read_tr16_b64_v4i16((LAS s16x4*)(L + a))); }
DI v4u cat(v2u a, v2u b) { return v4u{a.x, a.y, b.x, b.y}; }

DI u32 swz(u32 row) { return ((row & 3) << 2) | ((row >> 2) & 3); }

template <int RB> DI v4u frag_tr(lds_t L, u32 base, u32 lane, u32 ks, u32 c0) {
    const u32 g = lane >> 4, q = (lane >> 2) & 3, p = lane & 3;
    const u32 r0 = 32 * ks + 8 * g + q, r1 = r0 + 4, ch = c0 + (p >> 1);
    return cat(lds_tr(L, base + r0 * RB + 16 * (ch ^ swz(r0)) + 8 * (p & 1)), lds_tr(L, base + r1 * RB + 16 * (ch ^ swz(r1)) + 8 * (p & 1)));
}

DI void wait_tag(rsrc_t rs, int off, v2u tag, bool active) {
    const int o = active ? off : OOB;
#pragma unroll 1
    for (int n = 0; n < SPIN_CAP; ++n) {
        fence_compiler();
        const v2u f = ldw64(rs, o);
        if (__all(!active || (f.x == tag.x && f.y == tag.y))) break;
        __builtin_amdgcn_s_sleep(1);
    }
    fence_compiler();
}

template <bool TAGGED> DI bool pending(v4u a, v2u t) { return TAGGED ? (a.y != t.x || a.w != t.y) : umax4(a) == SENT; }
template <bool TAGGED, int N> DI bool all_valid(const v4u (&a)[N], v2u t) {
    bool bad = false;
#pragma unroll
    for (int i = 0; i < N; ++i) bad |= pending<TAGGED>(a[i], t);
    return !__any(bad);
}
template <int N> DI void gather_issue(rsrc_t rs, const int (&off)[N], v4u (&a)[N]) {
#pragma unroll
    for (int i = 0; i < N; ++i) a[i] = ldw128(rs, off[i]);
}
template <bool TAGGED, int N, int NAP = 0> DI void gather_retry(rsrc_t rs, const int (&off)[N], v4u (&a)[N], v2u tg) {
#pragma unroll 1
    for (int it = 0; it < SPIN_CAP; ++it) {
        if (NAP) __builtin_amdgcn_s_sleep(NAP);
        fence_compiler();
        v4u t[N];
        bool bad[N];
#pragma unroll
        for (int i = 0; i < N; ++i) { bad[i] = pending<TAGGED>(a[i], tg); t[i] = ldw128(rs, bad[i] ? off[i] : OOB); }
#pragma unroll
        for (int i = 0; i < N; ++i) a[i] = v4u{bad[i] ? t[i].x : a[i].x, bad[i] ? t[i].y : a[i].y, bad[i] ? t[i].z : a[i].z, bad[i] ? t[i].w : a[i].w};
        if (all_valid<TAGGED>(a, tg)) break;
    }
    fence_compiler();
}

constexpr u32 XN_STRIDE = 1088, QS_STRIDE = 320;

DI v4u layernorm_row(v4u xv) {
    float f[8] = {bf_lo(xv.x), bf_hi(xv.x), bf_lo(xv.y), bf_hi(xv.y), bf_lo(xv.z), bf_hi(xv.z), bf_lo(xv.w), bf_hi(xv.w)};
    float s = ((f[0] + f[1]) + (f[2] + f[3])) + ((f[4] + f[5]) + (f[6] + f[7]));
    const float mean = wave_sum(s) * (1.0f / 512.0f);
    float v = 0.f;
#pragma unroll
    for (int i = 0; i < 8; ++i) { f[i] -= mean; v += f[i] * f[i]; }
    const float rstd = 1.0f / sqrtf(wave_sum(v) * (1.0f / 512.0f) + 1e-5f);
    return v4u{pk(f[0] * rstd, f[1] * rstd), pk(f[2] * rstd, f[3] * rstd), pk(f[4] * rstd, f[5] * rstd), pk(f[6] * rstd, f[7] * rstd)};
}

DI void row_partial_words(const f32x4 (&acc)[8], v2u t2, u32 lane, v4u (&o)[2]) {
    const u32 k = (lane & 15) >> 2;
#pragma unroll
    for (int u = 0; u < 2; ++u) {
        const f32x4 v = sel4(k & 2, sel4(k & 1, acc[4 * u + 3], acc[4 * u + 2]), sel4(k & 1, acc[4 * u + 1], acc[4 * u]));
        o[u] = v4u{pk(v.x, v.y), t2.x, pk(v.z, v.w), t2.y};
    }
}
DI void store_row_partials(rsrc_t rs, int region, int slots, int slot, const v4u (&o)[2], u32 w, u32 lane) {
    const u32 n = lane & 15, g = lane >> 4, b = n & 3, k = n >> 2;
#pragma unroll
    for (int u = 0; u < 2; ++u) {
        const int f = 16 * b + 4 * w + 2 * u + (k >> 1);
        stw128(rs, region + (f * slots + (slot ^ (f & 7))) * 128 + 64 * (k & 1) + 16 * g, o[u]);
    }
}

DI u32 split_weight(v4u ml) {
    float mx = as_f(ml.x);
    mx = fmaxf(mx, ror<1>(mx)); mx = fmaxf(mx, ror<2>(mx)); mx = fmaxf(mx, ror<4>(mx));
    const float e = __builtin_amdgcn_exp2f(as_f(ml.x) - max32(max16(mx)));
    float den = e * as_f(ml.z);
    den += ror<1>(den); den += ror<2>(den); den += ror<4>(den);
    const float wgt = e * __builtin_amdgcn_rcpf(add32(add16(den)));
    return pk(wgt, wgt);
}
template <int I> DI v4u diag_frag(u32 wgt, const u32 (&dmask)[4]) {
    const u32 d = (u32)__builtin_amdgcn_update_dpp(0, (int)wgt, 0x150 + I, 0xf, 0xf, false);
    return v4u{d & dmask[0], d & dmask[1], d & dmask[2], d & dmask[3]};
}
DI void diag_frags(u32 wgt, const u32 (&dmask)[4], v4u (&d)[8]) {
    d[0] = diag_frag<0>(wgt, dmask); d[1] = diag_frag<1>(wgt, dmask); d[2] = diag_frag<2>(wgt, dmask); d[3] = diag_frag<3>(wgt, dmask);
    d[4] = diag_frag<4>(wgt, dmask); d[5] = diag_frag<5>(wgt, dmask); d[6] = diag_frag<6>(wgt, dmask); d[7] = diag_frag<7>(wgt, dmask);
    asm volatile("s_nop 1" : "+v"(d[0]), "+v"(d[1]), "+v"(d[2]), "+v"(d[3]), "+v"(d[4]), "+v"(d[5]), "+v"(d[6]), "+v"(d[7]));
}

DI u32 pair_row(u32 lane) { const u32 n = lane & 15; return 16 * (n >> 3) + 4 * (lane >> 4) + 2 * ((n >> 2) & 1); }
DI v4u pair_words(const f32x4 (&acc)[2], v2u t2, u32 lane) {
    const f32x4 v = sel4(lane & 8, acc[1], acc[0]);
    return v4u{as_u((lane & 4) ? v.z : v.x), t2.x, as_u((lane & 4) ? v.w : v.y), t2.y};
}
DI void gather_head_sum(rsrc_t rs, lds_t L, int region, u32 h, u32 dst, v2u t2, u32 w, u32 lane) {
    v4u a[4];
    int off[4];
#pragma unroll
    for (int i = 0; i < 4; ++i) off[i] = region + (h * 4 + i) * 4096 + w * 1024 + lane * 16;
    gather_issue(rs, off, a);
    if (!all_valid<true>(a, t2)) gather_retry<true>(rs, off, a, t2);
    *(LAS u32*)(L + dst + w * QS_STRIDE + 4 * lane) =
        pk((as_f(a[0].x) + as_f(a[1].x)) + (as_f(a[2].x) + as_f(a[3].x)), (as_f(a[0].z) + as_f(a[1].z)) + (as_f(a[2].z) + as_f(a[3].z)));
}
DI void mul_rows(lds_t L, u32 src, const v4u (&a)[2][4], f32x4 (&acc)[2], u32 lane) {
    v4u b[4];
#pragma unroll
    for (int ks = 0; ks < 4; ++ks) b[ks] = lds128(L, src + (lane & 3) * QS_STRIDE + 64 * ks + 16 * (lane >> 4));
    pin();
    mfma16_first(acc[0], a[0][0], b[0]);
    mfma16_first(acc[1], a[1][0], b[0]);
#pragma unroll
    for (int ks = 1; ks < 4; ++ks) { mfma16_acc(acc[0], a[0][ks], b[ks]); mfma16_acc(acc[1], a[1][ks], b[ks]); }
    mfma_settle(acc[0], acc[1]);
}

DI float gelu_tanh(float s) {
    const float u = 0.7978845608028654f * (s + 0.044715f * s * s * s);
    const float th = 1.0f - 2.0f / (__expf(2.0f * u) + 1.0f);
    return 0.5f * s * (1.0f + th);
}

DI void role_w(const Params& p, lds_t L, rsrc_t rs, u32 wi, u32 xcd) {
    STAMP_DECL;
    STAMP(0);
    const u32 tid = threadIdx.x, lane = tid & 63, w = __builtin_amdgcn_readfirstlane(tid >> 6);
    const u32 h = wi >> 2, j = wi & 3, n = lane & 15, g = lane >> 4;
    constexpr u32 L_WQ = 0, L_WUK = 32768, L_JUNK = 65536, L_WO = L_WUK, L_HI = 131072;
    constexpr u32 L_XN = L_HI, L_QS = L_HI + 8192, L_CC = 0, L_CX = 2048;
    const u32 slot0 = 8192 * w, src0 = (32 * w + g) * 8192;

    const u32 t_begin = W_START ? (u32)__builtin_amdgcn_s_memrealtime() : 0;
    u64 e64 = epoch_issue(p.ws);
    const u32 slot = wi & 15, rank = slot - (slot > 6 ? 2 : 0);
    const bool ffn = FFN_MAP ? (slot != 5 && slot != 6) : wi < NFFN;
    const u32 fc = FFN_MAP ? (wi < NFFN ? wi : 16 * (min(rank, 13u) >> 1) + 5 + (rank & 1)) : min(wi, (u32)(NFFN - 1));
    v4u xv = ld128_issue(p.x, tid * 16);
    v2u b1v = ld64p_issue(p.b1, 32 * fc + 8 * g);
#pragma unroll
    for (int i = 0; i < 8; ++i) dma_nt(p.wq + j * 1048576 + h * 256, src0 + 4 * i * 8192 + 16 * (n ^ swz(4 * i + g)), L_WQ + slot0 + 1024 * i);
    if (w == 0) stw128(rs, WS_QA + ((g * 32 + h) * 4 + j) * 256 + n * 16, v4u{SENT, SENT, SENT, SENT});
    else touch(p.ws, w == 1 ? WS_FLAGW + (lane & 7) * 1024 + (wi >> 1) * 16 : WS_HINT3 + wi * 128 + (lane & 7) * 16, L_JUNK + 4096 + w * 1024);
    if (TOUCH_QG) touch(p.ws, WS_QG + wi * 4096 + tid * 16, L_JUNK + w * 1024);
#pragma unroll
    for (int i = 0; i < 8; ++i) dma_nt(p.wuk + j * 1048576 + h * 256, src0 + 4 * i * 8192 + 16 * (n ^ ((4 * i + g) & 15)), L_WUK + slot0 + 1024 * i);
    asm volatile("s_waitcnt vmcnt(%2)" : "+v"(xv), "+v"(b1v) : "i"(17 + TOUCH_QG) : "memory");
    STAMP(1);
    *(LAS v4u*)(L + L_XN + w * XN_STRIDE + lane * 16) = layernorm_row(xv);
    e64 = epoch_wait(e64);
    const v2u tag = {(u32)e64, (u32)(e64 >> 32)}, t2 = {tag.x, tag.x ^ TAG_MIX};
    if (w == 0) wait_vm<8 + TOUCH_QG>();
    else wait_vm<9 + TOUCH_QG>();
    bar();
    STAMP(2);
    stw64(rs, lane < 2 ? WS_FLAGW + (2 * w + lane) * 1024 + wi * 8 : OOB, tag);
    {
        v4u xb[4], a[2][4];
#pragma unroll
        for (int ks = 0; ks < 4; ++ks) {
            xb[ks] = lds128(L, L_XN + (n & 3) * XN_STRIDE + 256 * j + 64 * ks + 16 * g);
            a[0][ks] = frag_tr<256>(L, L_WQ, lane, ks, 4 * w);
            a[1][ks] = frag_tr<256>(L, L_WQ, lane, ks, 4 * w + 2);
        }
        pin();
        f32x4 acc[2];
        mfma16_first(acc[0], a[0][0], xb[0]);
        mfma16_first(acc[1], a[1][0], xb[0]);
#pragma unroll
        for (int ks = 1; ks < 4; ++ks) { mfma16_acc(acc[0], a[0][ks], xb[ks]); mfma16_acc(acc[1], a[1][ks], xb[ks]); }
        mfma_settle(acc[0], acc[1]);
        stw128(rs, WS_QG + wi * 4096 + (n & 3) * 1024 + (32 * w + pair_row(lane)) * 8, pair_words(acc, t2, lane));
    }
    STAMP(3);
    wait_vm<2>();
    v4u uk[2][4];
#pragma unroll
    for (int t = 0; t < 2; ++t)
#pragma unroll
        for (int ks = 0; ks < 4; ++ks) uk[t][ks] = lds128(L, L_WUK + slot0 + (16 * t + n) * 256 + 16 * ((4 * ks + g) ^ n));
    if (QG_PACE) wait_vm<0>();
    STAMP(4);
    gather_head_sum(rs, L, WS_QG, h, L_QS, t2, w, lane);
    bar();
    STAMP(5);
    {
        f32x4 acc[2];
        mul_rows(L, L_QS, uk, acc, lane);
        const float sc = 0.08838834764831845f * 1.4426950408889634f;
        const f32x4 v = sel4(n & 4, acc[1], acc[0]);
        if (n < 8) stw64(rs, WS_QA + (((n & 3) * 32 + h) * 4 + j) * 256 + 64 * w + 32 * (n >> 2) + 8 * g, v2u{pk(v.x * sc, v.y * sc), pk(v.z * sc, v.w * sc)});
    }
    if (HELP_K == 1) touch_word(p.ckv, (wi * 128 + 32 * w + 24 + (lane >> 3)) * 1024 + (lane & 7) * 128, L_JUNK + 8192 + w * 1024);
    if (HELP_K == 2 && (h & 3) != 1) {
#pragma unroll
        for (int i = 0; i < 2; ++i) {
            const u32 ln = min(((slot - (slot > 7 ? 4 : 0)) * 4 + w) * 86 + min(64 * i + lane, 85u), 4095u);
            touch_word(p.ckv, (((wi & ~15u) + (ln >> 8)) * 128 + 32 * ((ln >> 6) & 3) + 24 + ((ln >> 3) & 7)) * 1024 + (ln & 7) * 128, L_JUNK + 8192 + w * 1024);
        }
    }
    if (HELP_K == 3 && (h & 3) != 1) {
        const u32 ln = min(((slot - (slot > 7 ? 4 : 0)) * 4 + w) * 43 + min(lane, 42u), 2047u);
        touch_word(p.ckv, (((wi & ~15u) + (ln >> 7)) * 128 + 32 * ((ln >> 5) & 3) + 28 + ((ln >> 3) & 3)) * 1024 + (ln & 7) * 128, L_JUNK + 8192 + w * 1024);
    }
    STAMP(6);
    v4u xf[4];
#pragma unroll
    for (int k = 0; k < 4; ++k) xf[k] = lds128(L, L_XN + (n & 3) * XN_STRIDE + 64 * (4 * w + k) + 16 * g);
    if (W_SLEEP) __builtin_amdgcn_s_sleep(W_SLEEP);
    if (W_START) {
#pragma unroll 1
        for (int it = 0; it < 64 && (u32)__builtin_amdgcn_s_memrealtime() - t_begin < W_START; ++it) __builtin_amdgcn_s_sleep(2);
    }
    if (USE_HINT1 != 0 && w == 0) wait_tag(rs, WS_HINT1 + wi * 128, tag, lane == 0);
    bar();
    STAMP(7);
    {
        const u32 l_w1 = 8192 * w, l_w2 = l_w1 + 4096;
        const u32 r = (lane >> 1) ^ ((lane >> 2) & 4), q = (lane >> 2) & 3, pp = lane & 3;
        if (ffn) {
#pragma unroll
            for (int i = 0; i < 4; ++i) dma_nt(p.w1 + 32 * fc, (128 * w + 32 * i + r) * 3584 + 16 * (lane & 1), l_w1 + 1024 * i);
#pragma unroll
            for (int i = 0; i < 4; ++i) dma_nt(p.w2 + 16384 * fc, (4 * i + g) * 1024 + 256 * w + 16 * (n ^ (((4 * i + g) & 7) << 1)), l_w2 + 1024 * i);
        }
#pragma unroll
        for (int i = 0; i < 8; ++i) dma_nt(p.wuv + j * 1048576 + h * 256, src0 + 4 * i * 8192 + 16 * (n ^ swz(4 * i + g)), L_HI + slot0 + 1024 * i);
        wait_vm<12>();
        STAMP(13);
        f32x4 acc = {0.f, 0.f, 0.f, 0.f};
#pragma unroll
        for (int k = 0; k < 4; ++k) {
            const u32 a0 = l_w1 + (32 * k + 8 * g + (q ^ ((g & 1) << 2))) * 32 + 8 * pp;
            acc = mfma16(cat(lds_tr(L, a0), lds_tr(L, a0 ^ 128)), xf[k], acc);
        }
        if (n < 4) *(LAS f32x4*)(L + l_w1 + n * 64 + 16 * g) = acc;
#pragma unroll
        for (int i = 0; i < 8; ++i) dma_nt(p.wo + h * 131072 + j * 256, (32 * w + 4 * i + g) * 1024 + 16 * (n ^ swz(4 * i + g)), L_WO + slot0 + 1024 * i);
        if (TOUCH_CX) {
            touch(p.ws, WS_CX + wi * 4096 + tid * 16, L_JUNK + w * 1024);
            touch(p.ws, WS_SP + (g * 16 + 4 * j + w) * 8192 + h * 256 + n * 16, L_JUNK + 4096 + w * 1024);
        }
        wait_vm<16 + 2 * TOUCH_CX>();
        bar();
        f32x4 s = {bf_lo(b1v.x), bf_hi(b1v.x), bf_lo(b1v.y), bf_hi(b1v.y)};
#pragma unroll
        for (int ww = 0; ww < 4; ++ww) s += *(LAS f32x4*)(L + 8192 * ww + (n & 3) * 64 + 16 * g);
        v2u w2f[8];
#pragma unroll
        for (int t = 0; t < 8; ++t) w2f[t] = lds_tr(L, l_w2 + (4 * g + q) * 256 + 16 * ((2 * t + (pp >> 1)) ^ (((4 * g + q) & 7) << 1)) + 8 * (pp & 1));
        pin();
        const v2u hb = {pk(gelu_tanh(s.x), gelu_tanh(s.y)), pk(gelu_tanh(s.z), gelu_tanh(s.w))};
        f32x4 y[8];
#pragma unroll
        for (int t = 0; t < 8; ++t) y[t] = mfma16k16(w2f[t], hb, f32x4{0.f, 0.f, 0.f, 0.f});
        v4u yw[2];
        row_partial_words(y, t2, lane, yw);
        store_row_partials(rs, ffn ? WS_YP : OOB - YP_SPAN, NFFN, fc, yw, w, lane);
        STAMP(14);
    }
    wait_vm<2 + 2 * TOUCH_CX>();
    bar();
    STAMP(8);
    v4u uv[2][4], wo[2][4];
#pragma unroll
    for (int t = 0; t < 2; ++t)
#pragma unroll
        for (int ks = 0; ks < 4; ++ks) {
            uv[t][ks] = frag_tr<256>(L, L_HI, lane, ks, 4 * w + 2 * t);
            wo[t][ks] = frag_tr<256>(L, L_WO, lane, ks, 4 * w + 2 * t);
        }
    int cx_off = WS_CX + wi * 4096 + (n & 3) * 1024 + (32 * w + pair_row(lane)) * 8;
    int sp_off = WS_SP + ((n & 3) * 16 + 4 * j + w) * 8192 + h * 256 + pair_row(lane) * 8;
    int h3_off = tid == 0 ? WS_HINT3 + wi * 128 : OOB;
    keep(cx_off); keep(sp_off); keep(h3_off);
    u32 dmask[4];
#pragma unroll
    for (int t = 0; t < 4; ++t) { dmask[t] = (n >> 1) == (u32)t ? ((n & 1) ? 0xFFFF0000u : 0x0000FFFFu) : 0u; keep(dmask[t]); }
    int off[8];
#pragma unroll
    for (int i = 0; i < 8; ++i) off[i] = WS_OC + wi * 32768 + w * 8192 + (8 * g + i) * 256 + n * 16;
    keep(off[0]);
    const int ml_off[1] = {(int)(WS_ML + (w * 32 + ((8 * g + (n & 7)) ^ (h & 3))) * 512 + h * 16)};
    const int fk_off = WS_FLAGK + xcd * 4096 + j * 1024 + (32 * w + (lane & 31)) * 8;
    bool all_in = false;
#pragma unroll 1
    for (int it = 0; it < SPIN_CAP; ++it) {
        fence_compiler();
        const v2u f = ldw64(rs, fk_off);
        const bool ok = f.x == tag.x && f.y == tag.y;
        all_in = __all(ok);
        if (__any(ok)) break;
        __builtin_amdgcn_s_sleep(2);
    }
    fence_compiler();
    f32x4 cc;
    {
        v4u a[8], d[8], ml[1];
        gather_issue(rs, ml_off, ml);
        if (!all_in) {
            if (!all_valid<true>(ml, t2)) gather_retry<true, 1, ML_NAP>(rs, ml_off, ml, t2);
            diag_frags(split_weight(ml[0]), dmask, d);
            wait_tag(rs, fk_off, tag, true);
            STAMP(9);
            gather_issue(rs, off, a);
        } else {
            STAMP(9);
            gather_issue(rs, off, a);
            diag_frags(split_weight(ml[0]), dmask, d);
        }
        f32x4 c2;
        mfma16_first(cc, d[0], a[0]);
        mfma16_first(c2, d[1], a[1]);
#pragma unroll
        for (int i = 2; i < 8; i += 2) { mfma16_acc(cc, d[i], a[i]); mfma16_acc(c2, d[i + 1], a[i + 1]); }
        mfma_settle(cc, c2);
        cc += c2;
    }
    keep(cc);

    STAMP(10);
    if (lane < 32) *(LAS v2u*)(L + L_CC + w * QS_STRIDE + 16 * n + 8 * g) = v2u{pk(cc.x, cc.y), pk(cc.z, cc.w)};
    bar();
    STAMP(11);
    {
        f32x4 acc[2];
        mul_rows(L, L_CC, uv, acc, lane);
        stw128(rs, cx_off, pair_words(acc, t2, lane));
        if (H3_EARLY) stw64(rs, h3_off, tag);
        if (CX_PACE == 1) { if (H3_EARLY) wait_vm<1>(); else wait_vm<0>(); }
        if (CX_PACE > 1) __builtin_amdgcn_s_sleep(CX_PACE);
        if (!H3_EARLY) stw64(rs, h3_off, tag);
        gather_head_sum(rs, L, WS_CX, h, L_CX, t2, w, lane);
        bar();
        STAMP(15);
        mul_rows(L, L_CX, wo, acc, lane);
        stw128(rs, sp_off, pair_words(acc, t2, lane));
    }
    STAMP(12);
    STAMP_OUT();
}

DI void reduce_partials(rsrc_t rs, lds_t L, int base, u32 red_off, v2u t2, u32 w, u32 lane, bool last) {
    v4u a[4];
    int off[4];
#pragma unroll
    for (int i = 0; i < 4; ++i) off[i] = base + ((i == 3 && !last) ? 0 : i * 4096) + w * 1024 + lane * 16;
    gather_issue(rs, off, a);
    if (!all_valid<true>(a, t2)) gather_retry<true>(rs, off, a, t2);
    if (!last) a[3].x = a[3].z = 0;
    f32x4 s = {(bf_lo(a[0].x) + bf_lo(a[1].x)) + (bf_lo(a[2].x) + bf_lo(a[3].x)), (bf_hi(a[0].x) + bf_hi(a[1].x)) + (bf_hi(a[2].x) + bf_hi(a[3].x)),
               (bf_lo(a[0].z) + bf_lo(a[1].z)) + (bf_lo(a[2].z) + bf_lo(a[3].z)), (bf_hi(a[0].z) + bf_hi(a[1].z)) + (bf_hi(a[2].z) + bf_hi(a[3].z))};
#pragma unroll
    for (int e = 0; e < 4; ++e) s[e] = add32(add16(s[e] + ror<8>(s[e])));
    if (lane < 8) *(LAS f32x4*)(L + red_off + w * 128 + lane * 16) = s;
}

DI void role_k(const Params& p, lds_t L, rsrc_t rs, u32 ki, u32 xcd) {
    STAMP_DECL;
    STAMP(0);
    const u32 tid = threadIdx.x, lane = tid & 63, w = __builtin_amdgcn_readfirstlane(tid >> 6);
    const u32 b = ki >> 5, sp = ki & 31, n = lane & 15, g = lane >> 4, q = (lane >> 2) & 3, pp = lane & 3;
    const u32 f = ki >> 1;
    constexpr u32 L_C = 0, L_Q = 131072, L_P = L_Q, L_MX = L_Q + 8192;
    int ml_off = (lane >> 3) == w ? WS_ML + ki * 512 + lane * 16 : OOB;

    u64 e64 = epoch_issue(p.ws);
    if (HELP_W) {
        const char* src = p.wq + (32 * (ki & 15) + 8 * w + (lane >> 3)) * 8192 + (ki >> 4) * 1024 + (lane & 7) * 128;
        asm volatile("s_mov_b32 m0, %0\n\ts_nop 0\n\tglobal_load_lds_dword %1, off" :: "s"(L_Q + 4096 + 8192 * w), "v"(src) : "memory");
    }
    if (K_SLEEP) __builtin_amdgcn_s_sleep(K_SLEEP);
    u32 xo = ld32_issue(p.x, (32 * f + 8 * w + 2 * (lane & 3)) * 2);
    u32 bo = ld32_issue(p.b2, (32 * (f & 15) + 8 * w + 2 * (lane & 3)) * 2);
    {
        const u32 fk = WS_FLAGK + lane * 4096 + (tid >> 6) * 1024 + (ki >> 1) * 16, h1 = WS_HINT1 + ki * 128 + (lane & 7) * 16;
        touch(p.ws, lane < 8 ? fk : h1, L_Q + 8192 * w);
    }
    const char* cbase = p.ckv + (size_t)(b * 4096 + sp * 128) * 1024;
    const int fw_off = WS_FLAGW + xcd * 1024 + (32 * w + (lane & 31)) * 8;
#define DMA_C(i) dma_nt(cbase, (32 * w + (i)) * 1024 + 16 * (lane ^ swz(i)), L_C + (32 * w + (i)) * 1024)
#pragma unroll
    for (int i = 0; i < FW_AT; ++i) DMA_C(i);
    v2u fw = ld64_issue(p.ws, fw_off);
#pragma unroll
    for (int i = FW_AT; i < 32; ++i) DMA_C(i);
#undef DMA_C
    STAMP(1);
    e64 = epoch_wait(e64);
    const v2u tag = {(u32)e64, (u32)(e64 >> 32)}, t2 = {tag.x, tag.x ^ TAG_MIX};
    const u32 hd = lane & 31, hi = lane >> 5;
    u32 aa[8], qa[8], tv[8][4];
    int oc_off = WS_OC + (n * 4 + w) * 32768 + b * 8192 + (sp ^ (n & 3)) * 256 + 32 * (g & 1) + 16 * (g >> 1);
    int oc_off2 = oc_off + 64 * 32768;
    {
        const u32 keyr = 16 * ((hd >> 2) & 1) + 4 * (hd >> 3) + (hd & 3);
        const u32 ab = L_C + (32 * w + keyr) * 1024 + 16 * (hi ^ swz(keyr)), qb = L_Q + hd * 1024 + 16 * (hi ^ swz(hd));
        const u32 t0 = L_C + (8 * g + q) * 1024 + 256 * w + 16 * ((pp >> 1) ^ swz(8 * g + q)) + 8 * (pp & 1);
        const u32 t1 = L_C + (8 * g + q + 4) * 1024 + 256 * w + 16 * ((pp >> 1) ^ swz(8 * g + q + 4)) + 8 * (pp & 1);
#pragma unroll
        for (int i = 0; i < 8; ++i) {
            aa[i] = ab ^ (32 * i); qa[i] = qb ^ (32 * i);
            tv[i][0] = t0 ^ (32 * i); tv[i][1] = t1 ^ (32 * i); tv[i][2] = tv[i][0] + 65536; tv[i][3] = tv[i][1] + 65536;
            keep(aa[i]); keep(qa[i]); keep(tv[i][0]); keep(tv[i][1]); keep(tv[i][2]); keep(tv[i][3]);
        }
        keep(oc_off); keep(oc_off2); keep(ml_off);
    }
    u32 mx_rd = L_MX + hd * 4, mx_wr = mx_rd + w * 1024, p_wr[2], p_rd[4], q_wr[4];
#pragma unroll
    for (int i = 0; i < 2; ++i) { p_wr[i] = L_P + hd * 256 + 16 * ((4 * w + 2 * hi + i) ^ (hd & 15)); keep(p_wr[i]); }
#pragma unroll
    for (int i = 0; i < 4; ++i) { p_rd[i] = L_P + n * 256 + 16 * ((4 * i + g) ^ n); keep(p_rd[i]); }
#pragma unroll
    for (int i = 0; i < 4; ++i) { q_wr[i] = L_Q + (8 * w + i) * 1024 + 16 * (lane ^ swz(8 * w + i)); keep(q_wr[i]); }
    keep(mx_rd); keep(mx_wr);
    asm volatile("s_waitcnt vmcnt(%3)" : "+v"(fw), "+v"(xo), "+v"(bo) : "i"(32 - FW_AT) : "memory");
    STAMP(9);
    if (!__all(fw.x == tag.x && fw.y == tag.y)) wait_tag(rs, fw_off, tag, true);
    if (USE_HINT1 == 1 && w == (ki & 3) && lane == 0) stw64(rs, WS_HINT1 + ki * 128, tag);
    STAMP(2);
    wait_vm<Q_VMCNT>();
    if (Q_SLEEP) __builtin_amdgcn_s_sleep(Q_SLEEP);
    STAMP(3);
    {
        v4u qv[8];
        int off[8];
#pragma unroll
        for (int i = 0; i < 8; ++i) off[i] = WS_QA + (b * 32 + 8 * w + i) * 1024 + lane * 16;
        gather_issue(rs, off, qv);
        if (!all_valid<false>(qv, tag)) gather_retry<false>(rs, off, qv, tag);
        wait_vm<0>();
        STAMP(4);
        if (USE_HINT1 == 2 && w == (ki & 3) && lane == 0) stw64(rs, WS_HINT1 + ki * 128, tag);
#pragma unroll
        for (int i = 0; i < 8; ++i) *(LAS v4u*)(L + (i < 4 ? q_wr[i] : (q_wr[i & 3] ^ 16) + 4096)) = qv[i];
    }
    fence_compiler();

    f32x16 sa;
    {
        constexpr int LA = 4;
        v4u fa[LA + 1], fb[LA + 1];
#pragma unroll
        for (int s = 0; s < LA; ++s) fa[s] = lds128(L, aa[s]);
        bar();
        STAMP(5);
#pragma unroll
        for (int s = 0; s < LA; ++s) fb[s] = lds128(L, qa[s]);
#pragma unroll
        for (int s = 0; s < 32; ++s) {
            if (s + LA < 32) {
                fa[(s + LA) % (LA + 1)] = lds128(L, aa[(s + LA) & 7] + 256 * ((s + LA) >> 3));
                fb[(s + LA) % (LA + 1)] = lds128(L, qa[(s + LA) & 7] + 256 * ((s + LA) >> 3));
            }
            pin();
            if (s == 0) mfma32_first(sa, fa[0], fb[0]);
            else mfma32_acc(sa, fa[s % (LA + 1)], fb[s % (LA + 1)]);
            pin();
            if (s == 8) asm volatile("s_barrier" ::: "memory");
        }
    }
#define THALF(t, ks, hf) lds_tr(L, tv[t][(hf) + ((ks) & 2)] + 32768 * ((ks) & 1))
    v2u ta[PV_D + 1][2];
#pragma unroll
    for (int u = 0; u < PV_D; ++u) { ta[u][0] = THALF(u >> 2, u & 3, 0); ta[u][1] = THALF(u >> 2, u & 3, 1); }
    pin();
    asm volatile("s_nop 7\n\ts_nop 3" : "+v"(sa));
    float mloc = fmaxf(fmaxf(fmaxf(fmaxf(sa[0], sa[1]), sa[2]), fmaxf(fmaxf(sa[3], sa[4]), sa[5])), fmaxf(fmaxf(fmaxf(sa[6], sa[7]), sa[8]), fmaxf(fmaxf(sa[9], sa[10]), sa[11])));
    mloc = max32(fmaxf(fmaxf(fmaxf(sa[12], sa[13]), fmaxf(sa[14], sa[15])), mloc));
    *(LAS float*)(L + mx_wr) = mloc;
    bar();
    STAMP(6);
    const float mall = fmaxf(fmaxf(*(LAS float*)(L + mx_rd), *(LAS float*)(L + mx_rd + 1024)), fmaxf(*(LAS float*)(L + mx_rd + 2048), *(LAS float*)(L + mx_rd + 3072)));
    {
#pragma unroll
        for (int c = 0; c < 2; ++c) {
#pragma unroll
            for (int i = 8 * c; i < 8 * c + 8; ++i) sa[i] = __builtin_amdgcn_exp2f(sa[i] - mall);
            *(LAS v4u*)(L + p_wr[c]) = v4u{pk(sa[8 * c], sa[8 * c + 1]), pk(sa[8 * c + 2], sa[8 * c + 3]), pk(sa[8 * c + 4], sa[8 * c + 5]), pk(sa[8 * c + 6], sa[8 * c + 7])};
        }
        f32x2 l2[4];
#pragma unroll
        for (int i = 0; i < 4; ++i) l2[i] = f32x2{sa[4 * i], sa[4 * i + 1]} + f32x2{sa[4 * i + 2], sa[4 * i + 3]};
        const f32x2 l1 = (l2[0] + l2[1]) + (l2[2] + l2[3]);
        *(LAS float*)(L + mx_wr + 128) = add32(l1.x + l1.y);
    }
    bar();
    STAMP(7);

    v4u pb[2][4];
#pragma unroll
    for (int ks = 0; ks < 4; ++ks)
#pragma unroll
        for (int ht = 0; ht < 2; ++ht) pb[ht][ks] = lds128(L, p_rd[ks] + 4096 * ht);
    pin();
    float sl[4];
#pragma unroll
    for (int i = 0; i < 4; ++i) sl[i] = *(LAS float*)(L + mx_rd + 128 + 1024 * i);
    pin();
    {
        f32x4 acc[2][2];
        u32 pc[2][2][2];
        v4u oc[2];
#pragma unroll
        for (int t = 0; t < 9; ++t) {
#pragma unroll
            for (int i = 0; i < 8; ++i) {
                const int ks = i >> 1, ht = i & 1;
                if (t < 8) {
                    const v4u a = cat(ta[(4 * t + ks) % (PV_D + 1)][0], ta[(4 * t + ks) % (PV_D + 1)][1]);
                    if (ks == 0) mfma16_first(acc[t & 1][ht], a, pb[ht][0]);
                    else mfma16_acc(acc[t & 1][ht], a, pb[ht][ks]);
                } else if (i == 0) {
                    mfma_settle(acc[1][0], acc[1][1]);
                }
                pin();
                if (const int u = 4 * t + ks + PV_D; u < 32) ta[u % (PV_D + 1)][ht] = THALF(u >> 2, u & 3, ht);
                if (t == 1 && i == 0) stw128(rs, ml_off, v4u{as_u(mall), t2.x, as_u((sl[0] + sl[1]) + (sl[2] + sl[3])), t2.y});
                if (t > 0) {
                    const int u = (t - 1) & 1;
                    if (i == 3 || i == 4) {
                        const f32x4 c = acc[u][i - 3];
                        pc[u][i - 3][0] = pk(c.x, c.y);
                        pc[u][i - 3][1] = pk(c.z, c.w);
                    }
                    if (u == 1 && (i == 5 || i == 6)) {
                        const auto r0 = __builtin_amdgcn_permlane16_swap(pc[0][i - 5][0], pc[1][i - 5][0], false, false);
                        const auto r1 = __builtin_amdgcn_permlane16_swap(pc[0][i - 5][1], pc[1][i - 5][1], false, false);
                        oc[i - 5] = v4u{r0[0], r1[0], r0[1], r1[1]};
                        if (PV_NOSTORE) { keep(oc[i - 5]); } else if (i == 5 || t == 8) stw128(rs, (i == 5 ? oc_off : oc_off2) + 64 * ((t - 2) >> 1), oc[i - 5]);
                    }
                    if (!PV_NOSTORE && u == 0 && t > 1 && i == 5) stw128(rs, oc_off2 + 64 * ((t - 3) >> 1), oc[1]);
                }
                pin();
            }
        }
    }
#undef THALF
    STAMP(8);
    wait_vm<0>();
    if (lane < 8) stw64(rs, WS_FLAGK + lane * 4096 + w * 1024 + ki * 8, tag);
    STAMP(15);

    constexpr u32 L_RED = L_Q + 16384;
    if (!(ki & 1)) {
#pragma unroll 1
        for (int it = 0; it < SPIN_CAP; ++it) {
            fence_compiler();
            if (__builtin_amdgcn_readfirstlane(ldw32(rs, WS_YP + f * (NFFN * 128) + ((f + 28 * w) % NFFN) * 128 + 4)) == t2.x) break;
            __builtin_amdgcn_s_sleep(4);
        }
        reduce_partials(rs, L, WS_YP + f * (NFFN * 128), L_RED + 1024, t2, w, lane, w < 2);
        bar();
        STAMP(11);
        const u32 col = 8 * w + 2 * (lane & 3);
        f32x2 y2 = {bf_lo(bo), bf_hi(bo)};
#pragma unroll
        for (int i = 0; i < 4; ++i) y2 += *(LAS f32x2*)(L + L_RED + 1024 + 128 * i + col * 4);
        f32x2 gate = {y2.x / (1.0f + __expf(-y2.x)), y2.y / (1.0f + __expf(-y2.y))};
        int off[2];
#pragma unroll
        for (int i = 0; i < 2; ++i) off[i] = WS_SP + f * 8192 + i * 4096 + (lane >> 2) * 256 + 64 * w + 16 * (lane & 3);
        char* dst = p.out + (32 * f + col) * 2;
        keep(gate); keep(off[0]); keep(off[1]); keep(dst);
        wait_tag(rs, WS_HINT3 + ((ki + 32 * w) & 127) * 128, tag, lane == 0);
        STAMP(12);
        if (SLEEP_E) __builtin_amdgcn_s_sleep(SLEEP_E);
        v4u a[2];
        gather_issue(rs, off, a);
        if (!all_valid<true>(a, t2)) gather_retry<true>(rs, off, a, t2);
        STAMP(13);
        float s0 = as_f(a[0].x) + as_f(a[1].x), s1 = as_f(a[0].z) + as_f(a[1].z);
        s0 += ror<4>(s0); s1 += ror<4>(s1);
        s0 += ror<8>(s0); s1 += ror<8>(s1);
        s0 = add32(add16(s0)); s1 = add32(add16(s1));
        if (lane < 4) *(__attribute__((address_space(1))) u32*)dst = pk(bf_lo(xo) + s0 * gate.x, bf_hi(xo) + s1 * gate.y);
        if (ki == 0 && tid == 0) stw64(rs, WS_EPOCH, tag);
        STAMP(14);
    }
    STAMP_OUT();
}

DI void code_prefetch(u32 r) {
    u64 pc;
    int len_k, len_w;
    asm volatile("s_getpc_b64 %0\n.Lmla_code_begin:\n\ts_mov_b32 %1, .Lmla_code_end_k-.Lmla_code_begin\n\ts_mov_b32 %2, .Lmla_code_end_w-.Lmla_code_begin"
                 : "=s"(pc), "=s"(len_k), "=s"(len_w));
    const int len = max(len_k, len_w);
    const u32 lane = threadIdx.x & 63, w = __builtin_amdgcn_readfirstlane(threadIdx.x >> 6);
    const int off = min((int)(r * 1024 + w * 256 + (lane & 3) * 64), max(len - 4, 0));
    asm volatile("s_mov_b32 m0, %0\n\ts_nop 0\n\tglobal_load_lds_dword %1, %2" :: "s"(131072 + 6144 + 8192 * w), "v"(off), "s"(pc) : "memory");
}

__global__ __launch_bounds__(256, 1) void mla_ffn_decode(Params pa) {
    if (CODE_PF) code_prefetch(blockIdx.x >> 3);
    Params p = pa;
    asm volatile("" : "+s"(p.x), "+s"(p.ckv), "+s"(p.wq), "+s"(p.wuk), "+s"(p.wuv), "+s"(p.wo));
    asm volatile("" : "+s"(p.w1), "+s"(p.b1), "+s"(p.w2), "+s"(p.b2), "+s"(p.out), "+s"(p.ws));
    __shared__ __attribute__((aligned(16))) char smem[163840];
    lds_t L = (lds_t)smem;
    const rsrc_t rs = __builtin_amdgcn_make_buffer_rsrc(p.ws, 0, WS_END, 0x00020000);
    const u32 xcd = blockIdx.x & 7, r = blockIdx.x >> 3, idx = xcd * 16 + ((r + ROT) & 15);
    if (r >= 16) {
        role_k(p, L, rs, idx, xcd);
        asm volatile(".Lmla_code_end_k:");
    } else {
        role_w(p, L, rs, idx, xcd);
        asm volatile(".Lmla_code_end_w:");
    }
}

}

extern "C" void kernel_launch(void* const* d_in, const int* in_sizes, int n_in,
                              void* d_out, int out_size, void* d_ws, size_t ws_size,
                              hipStream_t stream) {
    mla::Params p;
    p.x = (const char*)d_in[0];
    p.ckv = (const char*)d_in[1];
    p.wq = (const char*)d_in[2];
    p.wuk = (const char*)d_in[3];
    p.wuv = (const char*)d_in[4];
    p.wo = (const char*)d_in[5];
    p.w1 = (const char*)d_in[7];
    p.b1 = (const char*)d_in[8];
    p.w2 = (const char*)d_in[9];
    p.b2 = (const char*)d_in[10];
    p.out = (char*)d_out;
    p.ws = (char*)d_ws + WS_BASE;
    mla::mla_ffn_decode<<<256, 256, 0, stream>>>(p);
}
```

```cpp
#include <hip/hip_runtime.h>
#include <hip/hip_bf16.h>

namespace mla {

typedef unsigned u32;
typedef unsigned long long u64;
typedef u32 v2u __attribute__((ext_vector_type(2)));
typedef u32 v4u __attribute__((ext_vector_type(4)));
typedef float f32x2 __attribute__((ext_vector_type(2)));
typedef float f32x4 __attribute__((ext_vector_type(4)));
typedef float f32x16 __attribute__((ext_vector_type(16)));
typedef short s16x4 __attribute__((ext_vector_type(4)));
typedef __bf16 bf16x2 __attribute__((ext_vector_type(2)));
typedef __bf16 bf16x8 __attribute__((ext_vector_type(8)));
#define LAS __attribute__((address_space(3)))
#define DI __device__ __forceinline__

constexpr int SC1 = 16;
constexpr u32 SENT = 0xFFFFFFFFu;
constexpr u32 TAG_MIX = 0x5A3C96E1u;
constexpr int SPIN_CAP = 1 << 18;
constexpr int NFFN = 112;
constexpr int OOB = 0x7FFFFF00;
constexpr int YP_SPAN = 64 * NFFN * 128;

constexpr int WS_EPOCH = 0;
constexpr int WS_FLAGW = 4096;
constexpr int WS_FLAGK = WS_FLAGW + 8192;
constexpr int WS_START = WS_FLAGK + 32768;
constexpr int WS_HINT = WS_START + 16384;
constexpr int WS_QA = 131072;
constexpr int WS_ML = WS_QA + 131072;
constexpr int WS_QG = 524288;
constexpr int WS_YP = 1048576;
constexpr int WS_SP = 2097152;
constexpr int WS_CX = WS_SP + 524288;
constexpr int WS_OC = 3145728;
constexpr int WS_END = WS_OC + 4194304;
static_assert(WS_ML + 131072 <= WS_YP, "workspace map");

constexpr u32 K_START_LO = 105, K_START_HI = 85;
constexpr u32 W_START = 425, W_ADVANCE = 50;
constexpr int FW_AT = 28;
constexpr int Q_VMCNT = 4;
constexpr int PV_D = 6;
constexpr int SUM_NAP = 32;

struct Params {
    const char* x; const char* ckv; const char* wq; const char* wuk; const char* wuv; const char* wo;
    const char* w1; const char* b1; const char* w2; const char* b2;
    char* out; char* ws;
};

template <int N> DI void wait_vm() { asm volatile("s_waitcnt vmcnt(%0)" :: "i"(N) : "memory"); }
DI void bar() { asm volatile("s_waitcnt lgkmcnt(0)\n\ts_barrier" ::: "memory"); }
DI void fence_compiler() { asm volatile("" ::: "memory"); }
DI void pin() { __builtin_amdgcn_sched_barrier(0); }
DI void epoch_issue(const char* ws, u64& e, u64& t) { asm volatile("s_memrealtime %1\n\ts_load_dwordx2 %0, %2, 0x0" : "=&s"(e), "=&s"(t) : "s"(ws) : "memory"); }
DI void epoch_wait(u64& e, u64& t) { asm volatile("s_waitcnt lgkmcnt(0)" : "+s"(e), "+s"(t) :: "memory"); e += 1; }

DI void dma_nt(const char* base, u32 voff, u32 lds) {
    asm volatile("s_mov_b32 m0, %0\n\ts_nop 0\n\tglobal_load_lds_dwordx4 %1, %2 nt" :: "s"(lds), "v"(voff), "s"(base) : "memory");
}
DI void touch(const char* base, u32 voff, u32 lds) {
    asm volatile("s_mov_b32 m0, %0\n\ts_nop 0\n\tglobal_load_lds_dwordx4 %1, %2 sc1" :: "s"(lds), "v"(voff), "s"(base) : "memory");
}
DI v2u ld64_issue(const char* base, u32 voff) { v2u r; asm volatile("global_load_dwordx2 %0, %1, %2 sc1" : "=v"(r) : "v"(voff), "s"(base) : "memory"); return r; }
DI v2u ld64p_issue(const char* base, u32 voff) { v2u r; asm volatile("global_load_dwordx2 %0, %1, %2" : "=v"(r) : "v"(voff), "s"(base) : "memory"); return r; }
DI u32 ld32_issue(const char* base, u32 voff) { u32 r; asm volatile("global_load_dword %0, %1, %2" : "=v"(r) : "v"(voff), "s"(base) : "memory"); return r; }
DI v4u ld128_issue(const char* base, u32 voff) { v4u r; asm volatile("global_load_dwordx4 %0, %1, %2" : "=v"(r) : "v"(voff), "s"(base) : "memory"); return r; }

typedef __amdgpu_buffer_rsrc_t rsrc_t;
DI v4u ldw128(rsrc_t rs, int off) { return __builtin_amdgcn_raw_buffer_load_b128(rs, off, 0, SC1); }
DI v2u ldw64(rsrc_t rs, int off) { return __builtin_amdgcn_raw_buffer_load_b64(rs, off, 0, SC1); }
DI u32 ldw32(rsrc_t rs, int off) { return __builtin_amdgcn_raw_buffer_load_b32(rs, off, 0, SC1); }
DI void stw128(rsrc_t rs, int off, v4u v) { __builtin_amdgcn_raw_buffer_store_b128(v, rs, off, 0, SC1); }
DI void stw64(rsrc_t rs, int off, v2u v) { __builtin_amdgcn_raw_buffer_store_b64(v, rs, off, 0, SC1); }

DI float as_f(u32 x) { return __builtin_bit_cast(float, x); }
DI u32 as_u(float x) { return __builtin_bit_cast(u32, x); }
DI float bf_lo(u32 x) { return as_f(x << 16); }
DI float bf_hi(u32 x) { return as_f(x & 0xFFFF0000u); }
DI u32 pk(float lo, float hi) { f32x2 f = {lo, hi}; return __builtin_bit_cast(u32, __builtin_convertvector(f, bf16x2)); }
DI f32x4 sel4(bool c, f32x4 a, f32x4 b) { return f32x4{c ? a.x : b.x, c ? a.y : b.y, c ? a.z : b.z, c ? a.w : b.w}; }
DI u32 umax4(v4u a) { return max(max(a.x, a.y), max(a.z, a.w)); }

template <int N> DI float ror(float x) { return as_f((u32)__builtin_amdgcn_update_dpp(0, (int)as_u(x), 0x120 + N, 0xf, 0xf, false)); }
DI float add16(float x) { auto r = __builtin_amdgcn_permlane16_swap(as_u(x), as_u(x), false, false); return as_f(r[0]) + as_f(r[1]); }
DI float add32(float x) { auto r = __builtin_amdgcn_permlane32_swap(as_u(x), as_u(x), false, false); return as_f(r[0]) + as_f(r[1]); }
DI float max16(float x) { auto r = __builtin_amdgcn_permlane16_swap(as_u(x), as_u(x), false, false); return fmaxf(as_f(r[0]), as_f(r[1])); }
DI float max32(float x) { auto r = __builtin_amdgcn_permlane32_swap(as_u(x), as_u(x), false, false); return fmaxf(as_f(r[0]), as_f(r[1])); }
DI float wave_sum(float x) { x += ror<8>(x); x += ror<4>(x); x += ror<2>(x); x += ror<1>(x); return add32(add16(x)); }

DI f32x4 mfma16(v4u a, v4u b, f32x4 c) { return __builtin_amdgcn_mfma_f32_16x16x32_bf16(__builtin_bit_cast(bf16x8, a), __builtin_bit_cast(bf16x8, b), c, 0, 0, 0); }
DI f32x4 mfma16k16(v2u a, v2u b, f32x4 c) { return __builtin_amdgcn_mfma_f32_16x16x16bf16_1k(__builtin_bit_cast(s16x4, a), __builtin_bit_cast(s16x4, b), c, 0, 0, 0); }

DI void mfma16_first(f32x4& c, v4u a, v4u b) { asm volatile("v_mfma_f32_16x16x32_bf16 %0, %1, %2, 0" : "=&v"(c) : "v"(a), "v"(b)); }
DI void mfma16_acc(f32x4& c, v4u a, v4u b) { asm volatile("v_mfma_f32_16x16x32_bf16 %0, %1, %2, %0" : "+v"(c) : "v"(a), "v"(b)); }
DI void mfma32_first(f32x16& c, v4u a, v4u b) { asm volatile("v_mfma_f32_32x32x16_bf16 %0, %1, %2, 0" : "=&v"(c) : "v"(a), "v"(b)); }
DI void mfma32_acc(f32x16& c, v4u a, v4u b) { asm volatile("v_mfma_f32_32x32x16_bf16 %0, %1, %2, %0" : "+v"(c) : "v"(a), "v"(b)); }
DI void mfma_settle(f32x4& c0, f32x4& c1) { asm volatile("s_nop 7" : "+v"(c0), "+v"(c1)); }
template <class T> DI void keep(T& x) { asm volatile("" : "+v"(x)); }

typedef LAS char* lds_t;
DI v4u lds128(lds_t L, u32 a) { return *(LAS v4u*)(L + a); }
DI v2u lds_tr(lds_t L, u32 a) { return __builtin_bit_cast(v2u, __builtin_amdgcn_ds_read_tr16_b64_v4i16((LAS s16x4*)(L + a))); }
DI v4u cat(v2u a, v2u b) { return v4u{a.x, a.y, b.x, b.y}; }

DI u32 swz(u32 row) { return ((row & 3) << 2) | ((row >> 2) & 3); }

template <int RB> DI v4u frag_tr(lds_t L, u32 base, u32 lane, u32 ks, u32 c0) {
    const u32 g = lane >> 4, q = (lane >> 2) & 3, p = lane & 3;
    const u32 r0 = 32 * ks + 8 * g + q, r1 = r0 + 4, ch = c0 + (p >> 1);
    return cat(lds_tr(L, base + r0 * RB + 16 * (ch ^ swz(r0)) + 8 * (p & 1)), lds_tr(L, base + r1 * RB + 16 * (ch ^ swz(r1)) + 8 * (p & 1)));
}

DI void wait_tag(rsrc_t rs, int off, v2u tag, bool active) {
    const int o = active ? off : OOB;
#pragma unroll 1
    for (int n = 0; n < SPIN_CAP; ++n) {
        fence_compiler();
        const v2u f = ldw64(rs, o);
        if (__all(!active || (f.x == tag.x && f.y == tag.y))) break;
        __builtin_amdgcn_s_sleep(1);
    }
    fence_compiler();
}

template <bool TAGGED> DI bool pending(v4u a, v2u t) { return TAGGED ? (a.y != t.x || a.w != t.y) : umax4(a) == SENT; }
template <bool TAGGED, int N> DI bool all_valid(const v4u (&a)[N], v2u t) {
    bool bad = false;
#pragma unroll
    for (int i = 0; i < N; ++i) bad |= pending<TAGGED>(a[i], t);
    return !__any(bad);
}
template <int N> DI void gather_issue(rsrc_t rs, const int (&off)[N], v4u (&a)[N]) {
#pragma unroll
    for (int i = 0; i < N; ++i) a[i] = ldw128(rs, off[i]);
}
template <bool TAGGED, int N, int NAP = 0> DI void gather_retry(rsrc_t rs, const int (&off)[N], v4u (&a)[N], v2u tg) {
#pragma unroll 1
    for (int it = 0; it < SPIN_CAP; ++it) {
        if (NAP) __builtin_amdgcn_s_sleep(NAP);
        fence_compiler();
        v4u t[N];
        bool bad[N];
#pragma unroll
        for (int i = 0; i < N; ++i) { bad[i] = pending<TAGGED>(a[i], tg); t[i] = ldw128(rs, bad[i] ? off[i] : OOB); }
#pragma unroll
        for (int i = 0; i < N; ++i) a[i] = v4u{bad[i] ? t[i].x : a[i].x, bad[i] ? t[i].y : a[i].y, bad[i] ? t[i].z : a[i].z, bad[i] ? t[i].w : a[i].w};
        if (all_valid<TAGGED>(a, tg)) break;
    }
    fence_compiler();
}

constexpr u32 XN_STRIDE = 1088, QS_STRIDE = 320;

DI v4u layernorm_row(v4u xv) {
    float f[8] = {bf_lo(xv.x), bf_hi(xv.x), bf_lo(xv.y), bf_hi(xv.y), bf_lo(xv.z), bf_hi(xv.z), bf_lo(xv.w), bf_hi(xv.w)};
    float s = ((f[0] + f[1]) + (f[2] + f[3])) + ((f[4] + f[5]) + (f[6] + f[7]));
    const float mean = wave_sum(s) * (1.0f / 512.0f);
    float v = 0.f;
#pragma unroll
    for (int i = 0; i < 8; ++i) { f[i] -= mean; v += f[i] * f[i]; }
    const float rstd = 1.0f / sqrtf(wave_sum(v) * (1.0f / 512.0f) + 1e-5f);
    return v4u{pk(f[0] * rstd, f[1] * rstd), pk(f[2] * rstd, f[3] * rstd), pk(f[4] * rstd, f[5] * rstd), pk(f[6] * rstd, f[7] * rstd)};
}

DI void row_partial_words(const f32x4 (&acc)[8], v2u t2, u32 lane, v4u (&o)[2]) {
    const u32 k = (lane & 15) >> 2;
#pragma unroll
    for (int u = 0; u < 2; ++u) {
        const f32x4 v = sel4(k & 2, sel4(k & 1, acc[4 * u + 3], acc[4 * u + 2]), sel4(k & 1, acc[4 * u + 1], acc[4 * u]));
        o[u] = v4u{pk(v.x, v.y), t2.x, pk(v.z, v.w), t2.y};
    }
}
DI void store_row_partials(rsrc_t rs, int region, int slots, int slot, const v4u (&o)[2], u32 w, u32 lane) {
    const u32 n = lane & 15, g = lane >> 4, b = n & 3, k = n >> 2;
#pragma unroll
    for (int u = 0; u < 2; ++u) {
        const int f = 16 * b + 4 * w + 2 * u + (k >> 1);
        stw128(rs, region + (f * slots + (slot ^ (f & 7))) * 128 + 64 * (k & 1) + 16 * g, o[u]);
    }
}

DI u32 split_weight(v4u ml) {
    float mx = as_f(ml.x);
    mx = fmaxf(mx, ror<1>(mx)); mx = fmaxf(mx, ror<2>(mx)); mx = fmaxf(mx, ror<4>(mx));
    const float e = __builtin_amdgcn_exp2f(as_f(ml.x) - max32(max16(mx)));
    float den = e * as_f(ml.z);
    den += ror<1>(den); den += ror<2>(den); den += ror<4>(den);
    const float wgt = e * __builtin_amdgcn_rcpf(add32(add16(den)));
    return pk(wgt, wgt);
}
template <int I> DI v4u diag_frag(u32 wgt, const u32 (&dmask)[4]) {
    const u32 d = (u32)__builtin_amdgcn_update_dpp(0, (int)wgt, 0x150 + I, 0xf, 0xf, false);
    return v4u{d & dmask[0], d & dmask[1], d & dmask[2], d & dmask[3]};
}
DI void diag_frags(u32 wgt, const u32 (&dmask)[4], v4u (&d)[8]) {
    d[0] = diag_frag<0>(wgt, dmask); d[1] = diag_frag<1>(wgt, dmask); d[2] = diag_frag<2>(wgt, dmask); d[3] = diag_frag<3>(wgt, dmask);
    d[4] = diag_frag<4>(wgt, dmask); d[5] = diag_frag<5>(wgt, dmask); d[6] = diag_frag<6>(wgt, dmask); d[7] = diag_frag<7>(wgt, dmask);
    asm volatile("s_nop 1" : "+v"(d[0]), "+v"(d[1]), "+v"(d[2]), "+v"(d[3]), "+v"(d[4]), "+v"(d[5]), "+v"(d[6]), "+v"(d[7]));
}

DI u32 pair_row(u32 lane) { const u32 n = lane & 15; return 16 * (n >> 3) + 4 * (lane >> 4) + 2 * ((n >> 2) & 1); }
DI v4u pair_words(const f32x4 (&acc)[2], v2u t2, u32 lane) {
    const f32x4 v = sel4(lane & 8, acc[1], acc[0]);
    return v4u{as_u((lane & 4) ? v.z : v.x), t2.x, as_u((lane & 4) ? v.w : v.y), t2.y};
}
DI void gather_head_sum(rsrc_t rs, lds_t L, int region, u32 h, u32 dst, v2u t2, u32 w, u32 lane) {
    v4u a[4];
    int off[4];
#pragma unroll
    for (int i = 0; i < 4; ++i) off[i] = region + (h * 4 + i) * 4096 + w * 1024 + lane * 16;
    gather_issue(rs, off, a);
    if (!all_valid<true>(a, t2)) gather_retry<true>(rs, off, a, t2);
    *(LAS u32*)(L + dst + w * QS_STRIDE + 4 * lane) =
        pk((as_f(a[0].x) + as_f(a[1].x)) + (as_f(a[2].x) + as_f(a[3].x)), (as_f(a[0].z) + as_f(a[1].z)) + (as_f(a[2].z) + as_f(a[3].z)));
}
DI void mul_rows(lds_t L, u32 src, const v4u (&a)[2][4], f32x4 (&acc)[2], u32 lane) {
    v4u b[4];
#pragma unroll
    for (int ks = 0; ks < 4; ++ks) b[ks] = lds128(L, src + (lane & 3) * QS_STRIDE + 64 * ks + 16 * (lane >> 4));
    pin();
    mfma16_first(acc[0], a[0][0], b[0]);
    mfma16_first(acc[1], a[1][0], b[0]);
#pragma unroll
    for (int ks = 1; ks < 4; ++ks) { mfma16_acc(acc[0], a[0][ks], b[ks]); mfma16_acc(acc[1], a[1][ks], b[ks]); }
    mfma_settle(acc[0], acc[1]);
}

DI float gelu_tanh(float s) {
    const float u = 0.7978845608028654f * (s + 0.044715f * s * s * s);
    const float th = 1.0f - 2.0f / (__expf(2.0f * u) + 1.0f);
    return 0.5f * s * (1.0f + th);
}

DI void role_w(const Params& p, lds_t L, rsrc_t rs, u32 wi, u32 xcd) {
    const u32 tid = threadIdx.x, lane = tid & 63, w = __builtin_amdgcn_readfirstlane(tid >> 6);
    const u32 h = wi >> 2, j = wi & 3, n = lane & 15, g = lane >> 4;
    constexpr u32 L_WQ = 0, L_WUK = 32768, L_JUNK = 65536, L_WO = L_WUK, L_HI = 131072;
    constexpr u32 L_XN = L_HI, L_QS = L_HI + 8192, L_CC = 0, L_CX = 2048;
    const u32 slot0 = 8192 * w, src0 = (32 * w + g) * 8192;

    u64 e64, t64;
    epoch_issue(p.ws, e64, t64);
    v4u xv = ld128_issue(p.x, tid * 16);
#pragma unroll
    for (int i = 0; i < 8; ++i) dma_nt(p.wq + j * 1048576 + h * 256, src0 + 4 * i * 8192 + 16 * (n ^ swz(4 * i + g)), L_WQ + slot0 + 1024 * i);
    if (w == 0) stw128(rs, WS_QA + ((g * 32 + h) * 4 + j) * 256 + n * 16, v4u{SENT, SENT, SENT, SENT});
    else touch(p.ws, w == 1 ? WS_FLAGW + (lane & 7) * 1024 + (wi >> 1) * 16 : WS_HINT + wi * 128 + (lane & 7) * 16, L_JUNK + 4096 + w * 1024);
    touch(p.ws, WS_QG + wi * 4096 + tid * 16, L_JUNK + w * 1024);
#pragma unroll
    for (int i = 0; i < 8; ++i) dma_nt(p.wuk + j * 1048576 + h * 256, src0 + 4 * i * 8192 + 16 * (n ^ ((4 * i + g) & 15)), L_WUK + slot0 + 1024 * i);
    asm volatile("s_waitcnt vmcnt(%1)" : "+v"(xv) : "i"(18) : "memory");
    *(LAS v4u*)(L + L_XN + w * XN_STRIDE + lane * 16) = layernorm_row(xv);
    epoch_wait(e64, t64);
    const u32 t_begin = (u32)t64;
    const v2u tag = {(u32)e64, (u32)(e64 >> 32)}, t2 = {tag.x, tag.x ^ TAG_MIX};
    if (w == 0) wait_vm<9>();
    else wait_vm<10>();
    bar();
    stw64(rs, lane < 2 ? WS_FLAGW + (2 * w + lane) * 1024 + wi * 8 : OOB, tag);
    {
        v4u xb[4], a[2][4];
#pragma unroll
        for (int ks = 0; ks < 4; ++ks) {
            xb[ks] = lds128(L, L_XN + (n & 3) * XN_STRIDE + 256 * j + 64 * ks + 16 * g);
            a[0][ks] = frag_tr<256>(L, L_WQ, lane, ks, 4 * w);
            a[1][ks] = frag_tr<256>(L, L_WQ, lane, ks, 4 * w + 2);
        }
        pin();
        f32x4 acc[2];
        mfma16_first(acc[0], a[0][0], xb[0]);
        mfma16_first(acc[1], a[1][0], xb[0]);
#pragma unroll
        for (int ks = 1; ks < 4; ++ks) { mfma16_acc(acc[0], a[0][ks], xb[ks]); mfma16_acc(acc[1], a[1][ks], xb[ks]); }
        mfma_settle(acc[0], acc[1]);
        stw128(rs, WS_QG + wi * 4096 + (n & 3) * 1024 + (32 * w + pair_row(lane)) * 8, pair_words(acc, t2, lane));
    }
    wait_vm<2>();
    v4u uk[2][4];
#pragma unroll
    for (int t = 0; t < 2; ++t)
#pragma unroll
        for (int ks = 0; ks < 4; ++ks) uk[t][ks] = lds128(L, L_WUK + slot0 + (16 * t + n) * 256 + 16 * ((4 * ks + g) ^ n));
    wait_vm<0>();
    const int t0_off = tid < 8 ? WS_START + xcd * 128 + lane * 8 : OOB;
    v2u t0 = ldw64(rs, t0_off);
    gather_head_sum(rs, L, WS_QG, h, L_QS, t2, w, lane);
    keep(t0);
    bar();
    {
        f32x4 acc[2];
        mul_rows(L, L_QS, uk, acc, lane);
        const float sc = 0.08838834764831845f * 1.4426950408889634f;
        const f32x4 v = sel4(n & 4, acc[1], acc[0]);
        if (n < 8) stw64(rs, WS_QA + (((n & 3) * 32 + h) * 4 + j) * 256 + 64 * w + 32 * (n >> 2) + 8 * g, v2u{pk(v.x * sc, v.y * sc), pk(v.z * sc, v.w * sc)});
    }
    u32 wj = wi;
    asm volatile("" : "+s"(wj));
    const u32 slot = wj & 15, rank = slot - (slot > 6 ? 2 : 0);
    const bool ffn = slot != 5 && slot != 6;
    const u32 fc = wj < NFFN ? wj : 16 * (min(rank, 13u) >> 1) + 5 + (rank & 1);
    v4u xf[4];
#pragma unroll
    for (int k = 0; k < 4; ++k) xf[k] = lds128(L, L_XN + (n & 3) * XN_STRIDE + 64 * (4 * w + k) + 16 * g);
    if (w == 0) {
        if (!__all(lane >= 8 || t0.y == t2.y)) {
#pragma unroll 1
            for (int it = 0; it < 16; ++it) {
                fence_compiler();
                t0 = ldw64(rs, t0_off);
                if (__all(lane >= 8 || t0.y == t2.y)) break;
                __builtin_amdgcn_s_sleep(4);
            }
            keep(t0);
        }
        float late = lane < 8 && t0.y == t2.y ? (float)(int)(t0.x - t_begin) : 0.f;
        late = fmaxf(late, ror<1>(late)); late = fmaxf(late, ror<2>(late)); late = fmaxf(late, ror<4>(late)); late = fmaxf(late, ror<8>(late));
        const u32 go = W_START - (ffn && ((h & 1) || j == 1) ? W_ADVANCE : 0) + __builtin_amdgcn_readfirstlane((u32)fminf(late, 300.f));
#pragma unroll 1
        for (int it = 0; it < 128 && (u32)__builtin_amdgcn_s_memrealtime() - t_begin < go; ++it) __builtin_amdgcn_s_sleep(2);
    }
    bar();
    {
        const u32 l_w1 = 8192 * w, l_w2 = l_w1 + 4096;
        const u32 r = (lane >> 1) ^ ((lane >> 2) & 4), q = (lane >> 2) & 3, pp = lane & 3;
        v2u b1v = ld64p_issue(p.b1, 32 * fc + 8 * g);
        if (ffn) {
#pragma unroll
            for (int i = 0; i < 4; ++i) dma_nt(p.w1 + 32 * fc, (128 * w + 32 * i + r) * 3584 + 16 * (lane & 1), l_w1 + 1024 * i);
#pragma unroll
            for (int i = 0; i < 4; ++i) dma_nt(p.w2 + 16384 * fc, (4 * i + g) * 1024 + 256 * w + 16 * (n ^ (((4 * i + g) & 7) << 1)), l_w2 + 1024 * i);
        }
#pragma unroll
        for (int i = 0; i < 8; ++i) dma_nt(p.wuv + j * 1048576 + h * 256, src0 + 4 * i * 8192 + 16 * (n ^ swz(4 * i + g)), L_HI + slot0 + 1024 * i);
        wait_vm<12>();
        f32x4 acc = {0.f, 0.f, 0.f, 0.f};
#pragma unroll
        for (int k = 0; k < 4; ++k) {
            const u32 a0 = l_w1 + (32 * k + 8 * g + (q ^ ((g & 1) << 2))) * 32 + 8 * pp;
            acc = mfma16(cat(lds_tr(L, a0), lds_tr(L, a0 ^ 128)), xf[k], acc);
        }
        if (n < 4) *(LAS f32x4*)(L + l_w1 + n * 64 + 16 * g) = acc;
#pragma unroll
        for (int i = 0; i < 8; ++i) dma_nt(p.wo + h * 131072 + j * 256, (32 * w + 4 * i + g) * 1024 + 16 * (n ^ swz(4 * i + g)), L_WO + slot0 + 1024 * i);
        touch(p.ws, WS_CX + wi * 4096 + tid * 16, L_JUNK + w * 1024);
        touch(p.ws, WS_SP + (g * 16 + 4 * j + w) * 8192 + h * 256 + n * 16, L_JUNK + 4096 + w * 1024);
        asm volatile("s_waitcnt vmcnt(%1)" : "+v"(b1v) : "i"(18) : "memory");
        bar();
        f32x4 s = {bf_lo(b1v.x), bf_hi(b1v.x), bf_lo(b1v.y), bf_hi(b1v.y)};
#pragma unroll
        for (int ww = 0; ww < 4; ++ww) s += *(LAS f32x4*)(L + 8192 * ww + (n & 3) * 64 + 16 * g);
        v2u w2f[8];
#pragma unroll
        for (int t = 0; t < 8; ++t) w2f[t] = lds_tr(L, l_w2 + (4 * g + q) * 256 + 16 * ((2 * t + (pp >> 1)) ^ (((4 * g + q) & 7) << 1)) + 8 * (pp & 1));
        pin();
        const v2u hb = {pk(gelu_tanh(s.x), gelu_tanh(s.y)), pk(gelu_tanh(s.z), gelu_tanh(s.w))};
        f32x4 y[8];
#pragma unroll
        for (int t = 0; t < 8; ++t) y[t] = mfma16k16(w2f[t], hb, f32x4{0.f, 0.f, 0.f, 0.f});
        v4u yw[2];
        row_partial_words(y, t2, lane, yw);
        store_row_partials(rs, ffn ? WS_YP : OOB - YP_SPAN, NFFN, fc, yw, w, lane);
    }
    wait_vm<4>();
    bar();
    v4u uv[2][4], wo[2][4];
#pragma unroll
    for (int t = 0; t < 2; ++t)
#pragma unroll
        for (int ks = 0; ks < 4; ++ks) {
            uv[t][ks] = frag_tr<256>(L, L_HI, lane, ks, 4 * w + 2 * t);
            wo[t][ks] = frag_tr<256>(L, L_WO, lane, ks, 4 * w + 2 * t);
        }
    int cx_off = WS_CX + wi * 4096 + (n & 3) * 1024 + (32 * w + pair_row(lane)) * 8;
    int sp_off = WS_SP + ((n & 3) * 16 + 4 * j + w) * 8192 + h * 256 + pair_row(lane) * 8;
    int h3_off = tid == 0 ? WS_HINT + wi * 128 : OOB;
    keep(cx_off); keep(sp_off); keep(h3_off);
    u32 dmask[4];
#pragma unroll
    for (int t = 0; t < 4; ++t) { dmask[t] = (n >> 1) == (u32)t ? ((n & 1) ? 0xFFFF0000u : 0x0000FFFFu) : 0u; keep(dmask[t]); }
    int off[8];
#pragma unroll
    for (int i = 0; i < 8; ++i) off[i] = WS_OC + wi * 32768 + w * 8192 + (8 * g + i) * 256 + n * 16;
    keep(off[0]);
    const int ml_off[1] = {(int)(WS_ML + (w * 32 + ((8 * g + (n & 7)) ^ (h & 3))) * 512 + h * 16)};
    const int fk_off = WS_FLAGK + xcd * 4096 + j * 1024 + (32 * w + (lane & 31)) * 8;
    bool all_in = false;
#pragma unroll 1
    for (int it = 0; it < SPIN_CAP; ++it) {
        fence_compiler();
        const v2u f = ldw64(rs, fk_off);
        const bool ok = f.x == tag.x && f.y == tag.y;
        all_in = __all(ok);
        if (__any(ok)) break;
        __builtin_amdgcn_s_sleep(2);
    }
    fence_compiler();
    f32x4 cc;
    {
        v4u a[8], d[8], ml[1];
        gather_issue(rs, ml_off, ml);
        if (!all_in) {
            if (!all_valid<true>(ml, t2)) gather_retry<true, 1, 2>(rs, ml_off, ml, t2);
            diag_frags(split_weight(ml[0]), dmask, d);
            wait_tag(rs, fk_off, tag, true);
            gather_issue(rs, off, a);
        } else {
            gather_issue(rs, off, a);
            diag_frags(split_weight(ml[0]), dmask, d);
        }
        f32x4 c2;
        mfma16_first(cc, d[0], a[0]);
        mfma16_first(c2, d[1], a[1]);
#pragma unroll
        for (int i = 2; i < 8; i += 2) { mfma16_acc(cc, d[i], a[i]); mfma16_acc(c2, d[i + 1], a[i + 1]); }
        mfma_settle(cc, c2);
        cc += c2;
    }
    keep(cc);

    if (lane < 32) *(LAS v2u*)(L + L_CC + w * QS_STRIDE + 16 * n + 8 * g) = v2u{pk(cc.x, cc.y), pk(cc.z, cc.w)};
    bar();
    {
        f32x4 acc[2];
        mul_rows(L, L_CC, uv, acc, lane);
        stw128(rs, cx_off, pair_words(acc, t2, lane));
        stw64(rs, h3_off, tag);
        wait_vm<1>();
        gather_head_sum(rs, L, WS_CX, h, L_CX, t2, w, lane);
        bar();
        mul_rows(L, L_CX, wo, acc, lane);
        stw128(rs, sp_off, pair_words(acc, t2, lane));
    }
}

DI void reduce_partials(rsrc_t rs, lds_t L, int base, u32 red_off, v2u t2, u32 w, u32 lane, bool last) {
    v4u a[4];
    int off[4];
#pragma unroll
    for (int i = 0; i < 4; ++i) off[i] = base + ((i == 3 && !last) ? 0 : i * 4096) + w * 1024 + lane * 16;
    gather_issue(rs, off, a);
    if (!all_valid<true>(a, t2)) gather_retry<true>(rs, off, a, t2);
    if (!last) a[3].x = a[3].z = 0;
    f32x4 s = {(bf_lo(a[0].x) + bf_lo(a[1].x)) + (bf_lo(a[2].x) + bf_lo(a[3].x)), (bf_hi(a[0].x) + bf_hi(a[1].x)) + (bf_hi(a[2].x) + bf_hi(a[3].x)),
               (bf_lo(a[0].z) + bf_lo(a[1].z)) + (bf_lo(a[2].z) + bf_lo(a[3].z)), (bf_hi(a[0].z) + bf_hi(a[1].z)) + (bf_hi(a[2].z) + bf_hi(a[3].z))};
#pragma unroll
    for (int e = 0; e < 4; ++e) s[e] = add32(add16(s[e] + ror<8>(s[e])));
    if (lane < 8) *(LAS f32x4*)(L + red_off + w * 128 + lane * 16) = s;
}

DI void role_k(const Params& p, lds_t L, rsrc_t rs, u32 ki, u32 xcd) {
    const u32 tid = threadIdx.x, lane = tid & 63, w = __builtin_amdgcn_readfirstlane(tid >> 6);
    const u32 b = ki >> 5, sp = ki & 31, n = lane & 15, g = lane >> 4, q = (lane >> 2) & 3, pp = lane & 3;
    const u32 f = ki >> 1;
    constexpr u32 L_C = 0, L_Q = 131072, L_P = L_Q, L_MX = L_Q + 8192;
    int ml_off = (lane >> 3) == w ? WS_ML + ki * 512 + lane * 16 : OOB;

    u64 e64, t64;
    epoch_issue(p.ws, e64, t64);
    epoch_wait(e64, t64);
    const u32 t_begin = (u32)t64;
    const v2u tag = {(u32)e64, (u32)(e64 >> 32)}, t2 = {tag.x, tag.x ^ TAG_MIX};
    stw64(rs, (ki & 15) == 0 && tid < 8 ? WS_START + lane * 128 + (ki >> 4) * 8 : OOB, v2u{t_begin, t2.y});
#pragma unroll 1
    for (int it = 0; it < 128 && (u32)__builtin_amdgcn_s_memrealtime() - t_begin < (xcd < 4 ? K_START_LO : K_START_HI); ++it) __builtin_amdgcn_s_sleep(1);
    u32 xo = ld32_issue(p.x, (32 * f + 8 * w + 2 * (lane & 3)) * 2);
    u32 bo = ld32_issue(p.b2, (32 * (f & 15) + 8 * w + 2 * (lane & 3)) * 2);
    touch(p.ws, WS_FLAGK + (lane & 7) * 4096 + (tid >> 6) * 1024 + (ki >> 1) * 16, L_Q + 8192 * w);
    const char* cbase = p.ckv + (size_t)(b * 4096 + sp * 128) * 1024;
    const int fw_off = WS_FLAGW + xcd * 1024 + (32 * w + (lane & 31)) * 8;
#define DMA_C(i) dma_nt(cbase, (32 * w + (i)) * 1024 + 16 * (lane ^ swz(i)), L_C + (32 * w + (i)) * 1024)
#pragma unroll
    for (int i = 0; i < FW_AT; ++i) DMA_C(i);
    v2u fw = ld64_issue(p.ws, fw_off);
#pragma unroll
    for (int i = FW_AT; i < 32; ++i) DMA_C(i);
#undef DMA_C
    const u32 hd = lane & 31, hi = lane >> 5;
    u32 aa[8], qa[8], tv[8][4];
    int oc_off = WS_OC + (n * 4 + w) * 32768 + b * 8192 + (sp ^ (n & 3)) * 256 + 32 * (g & 1) + 16 * (g >> 1);
    int oc_off2 = oc_off + 64 * 32768;
    {
        const u32 keyr = 16 * ((hd >> 2) & 1) + 4 * (hd >> 3) + (hd & 3);
        const u32 ab = L_C + (32 * w + keyr) * 1024 + 16 * (hi ^ swz(keyr)), qb = L_Q + hd * 1024 + 16 * (hi ^ swz(hd));
        const u32 t0 = L_C + (8 * g + q) * 1024 + 256 * w + 16 * ((pp >> 1) ^ swz(8 * g + q)) + 8 * (pp & 1);
        const u32 t1 = L_C + (8 * g + q + 4) * 1024 + 256 * w + 16 * ((pp >> 1) ^ swz(8 * g + q + 4)) + 8 * (pp & 1);
#pragma unroll
        for (int i = 0; i < 8; ++i) {
            aa[i] = ab ^ (32 * i); qa[i] = qb ^ (32 * i);
            tv[i][0] = t0 ^ (32 * i); tv[i][1] = t1 ^ (32 * i); tv[i][2] = tv[i][0] + 65536; tv[i][3] = tv[i][1] + 65536;
            keep(aa[i]); keep(qa[i]); keep(tv[i][0]); keep(tv[i][1]); keep(tv[i][2]); keep(tv[i][3]);
        }
        keep(oc_off); keep(oc_off2); keep(ml_off);
    }
    u32 mx_rd = L_MX + hd * 4, mx_wr = mx_rd + w * 1024, p_wr[2], p_rd[4], q_wr[4];
#pragma unroll
    for (int i = 0; i < 2; ++i) { p_wr[i] = L_P + hd * 256 + 16 * ((4 * w + 2 * hi + i) ^ (hd & 15)); keep(p_wr[i]); }
#pragma unroll
    for (int i = 0; i < 4; ++i) { p_rd[i] = L_P + n * 256 + 16 * ((4 * i + g) ^ n); keep(p_rd[i]); }
#pragma unroll
    for (int i = 0; i < 4; ++i) { q_wr[i] = L_Q + (8 * w + i) * 1024 + 16 * (lane ^ swz(8 * w + i)); keep(q_wr[i]); }
    keep(mx_rd); keep(mx_wr);
    asm volatile("s_waitcnt vmcnt(%3)" : "+v"(fw), "+v"(xo), "+v"(bo) : "i"(32 - FW_AT) : "memory");
    if (!__all(fw.x == tag.x && fw.y == tag.y)) wait_tag(rs, fw_off, tag, true);
    wait_vm<Q_VMCNT>();
    {
        v4u qv[8];
        int off[8];
#pragma unroll
        for (int i = 0; i < 8; ++i) off[i] = WS_QA + (b * 32 + 8 * w + i) * 1024 + lane * 16;
        gather_issue(rs, off, qv);
        if (!all_valid<false>(qv, tag)) gather_retry<false>(rs, off, qv, tag);
        wait_vm<0>();
#pragma unroll
        for (int i = 0; i < 8; ++i) *(LAS v4u*)(L + (i < 4 ? q_wr[i] : (q_wr[i & 3] ^ 16) + 4096)) = qv[i];
    }
    fence_compiler();

    f32x16 sa;
    {
        constexpr int LA = 4;
        v4u fa[LA + 1], fb[LA + 1];
#pragma unroll
        for (int s = 0; s < LA; ++s) fa[s] = lds128(L, aa[s]);
        bar();
#pragma unroll
        for (int s = 0; s < LA; ++s) fb[s] = lds128(L, qa[s]);
#pragma unroll
        for (int s = 0; s < 32; ++s) {
            if (s + LA < 32) {
                fa[(s + LA) % (LA + 1)] = lds128(L, aa[(s + LA) & 7] + 256 * ((s + LA) >> 3));
                fb[(s + LA) % (LA + 1)] = lds128(L, qa[(s + LA) & 7] + 256 * ((s + LA) >> 3));
            }
            pin();
            if (s == 0) mfma32_first(sa, fa[0], fb[0]);
            else mfma32_acc(sa, fa[s % (LA + 1)], fb[s % (LA + 1)]);
            pin();
            if (s == 8) asm volatile("s_barrier" ::: "memory");
        }
    }
#define THALF(t, ks, hf) lds_tr(L, tv[t][(hf) + ((ks) & 2)] + 32768 * ((ks) & 1))
    v2u ta[PV_D + 1][2];
#pragma unroll
    for (int u = 0; u < PV_D; ++u) { ta[u][0] = THALF(u >> 2, u & 3, 0); ta[u][1] = THALF(u >> 2, u & 3, 1); }
    pin();
    asm volatile("s_nop 7\n\ts_nop 3" : "+v"(sa));
    float mloc = fmaxf(fmaxf(fmaxf(fmaxf(sa[0], sa[1]), sa[2]), fmaxf(fmaxf(sa[3], sa[4]), sa[5])), fmaxf(fmaxf(fmaxf(sa[6], sa[7]), sa[8]), fmaxf(fmaxf(sa[9], sa[10]), sa[11])));
    mloc = max32(fmaxf(fmaxf(fmaxf(sa[12], sa[13]), fmaxf(sa[14], sa[15])), mloc));
    *(LAS float*)(L + mx_wr) = mloc;
    bar();
    const float mall = fmaxf(fmaxf(*(LAS float*)(L + mx_rd), *(LAS float*)(L + mx_rd + 1024)), fmaxf(*(LAS float*)(L + mx_rd + 2048), *(LAS float*)(L + mx_rd + 3072)));
    {
#pragma unroll
        for (int c = 0; c < 2; ++c) {
#pragma unroll
            for (int i = 8 * c; i < 8 * c + 8; ++i) sa[i] = __builtin_amdgcn_exp2f(sa[i] - mall);
            *(LAS v4u*)(L + p_wr[c]) = v4u{pk(sa[8 * c], sa[8 * c + 1]), pk(sa[8 * c + 2], sa[8 * c + 3]), pk(sa[8 * c + 4], sa[8 * c + 5]), pk(sa[8 * c + 6], sa[8 * c + 7])};
        }
        f32x2 l2[4];
#pragma unroll
        for (int i = 0; i < 4; ++i) l2[i] = f32x2{sa[4 * i], sa[4 * i + 1]} + f32x2{sa[4 * i + 2], sa[4 * i + 3]};
        const f32x2 l1 = (l2[0] + l2[1]) + (l2[2] + l2[3]);
        *(LAS float*)(L + mx_wr + 128) = add32(l1.x + l1.y);
    }
    bar();

    v4u pb[2][4];
#pragma unroll
    for (int ks = 0; ks < 4; ++ks)
#pragma unroll
        for (int ht = 0; ht < 2; ++ht) pb[ht][ks] = lds128(L, p_rd[ks] + 4096 * ht);
    pin();
    float sl[4];
#pragma unroll
    for (int i = 0; i < 4; ++i) sl[i] = *(LAS float*)(L + mx_rd + 128 + 1024 * i);
    pin();
    {
        f32x4 acc[2][2];
        u32 pc[2][2][2];
        v4u oc[2];
#pragma unroll
        for (int t = 0; t < 9; ++t) {
#pragma unroll
            for (int i = 0; i < 8; ++i) {
                const int ks = i >> 1, ht = i & 1;
                if (t < 8) {
                    const v4u a = cat(ta[(4 * t + ks) % (PV_D + 1)][0], ta[(4 * t + ks) % (PV_D + 1)][1]);
                    if (ks == 0) mfma16_first(acc[t & 1][ht], a, pb[ht][0]);
                    else mfma16_acc(acc[t & 1][ht], a, pb[ht][ks]);
                } else if (i == 0) {
                    mfma_settle(acc[1][0], acc[1][1]);
                }
                pin();
                if (const int u = 4 * t + ks + PV_D; u < 32) ta[u % (PV_D + 1)][ht] = THALF(u >> 2, u & 3, ht);
                if (t == 1 && i == 0) stw128(rs, ml_off, v4u{as_u(mall), t2.x, as_u((sl[0] + sl[1]) + (sl[2] + sl[3])), t2.y});
                if (t > 0) {
                    const int u = (t - 1) & 1;
                    if (i == 3 || i == 4) {
                        const f32x4 c = acc[u][i - 3];
                        pc[u][i - 3][0] = pk(c.x, c.y);
                        pc[u][i - 3][1] = pk(c.z, c.w);
                    }
                    if (u == 1 && (i == 5 || i == 6)) {
                        const auto r0 = __builtin_amdgcn_permlane16_swap(pc[0][i - 5][0], pc[1][i - 5][0], false, false);
                        const auto r1 = __builtin_amdgcn_permlane16_swap(pc[0][i - 5][1], pc[1][i - 5][1], false, false);
                        oc[i - 5] = v4u{r0[0], r1[0], r0[1], r1[1]};
                        if (i == 5 || t == 8) stw128(rs, (i == 5 ? oc_off : oc_off2) + 64 * ((t - 2) >> 1), oc[i - 5]);
                    }
                    if (u == 0 && t > 1 && i == 5) stw128(rs, oc_off2 + 64 * ((t - 3) >> 1), oc[1]);
                }
                pin();
            }
        }
    }
#undef THALF
    wait_vm<0>();
    if (lane < 8) stw64(rs, WS_FLAGK + lane * 4096 + w * 1024 + ki * 8, tag);

    constexpr u32 L_RED = L_Q + 16384;
    if (!(ki & 1)) {
#pragma unroll 1
        for (int it = 0; it < SPIN_CAP; ++it) {
            fence_compiler();
            if (__builtin_amdgcn_readfirstlane(ldw32(rs, WS_YP + f * (NFFN * 128) + ((f + 28 * w) % NFFN) * 128 + 4)) == t2.x) break;
            __builtin_amdgcn_s_sleep(4);
        }
        reduce_partials(rs, L, WS_YP + f * (NFFN * 128), L_RED + 1024, t2, w, lane, w < 2);
        bar();
        const u32 col = 8 * w + 2 * (lane & 3);
        f32x2 y2 = {bf_lo(bo), bf_hi(bo)};
#pragma unroll
        for (int i = 0; i < 4; ++i) y2 += *(LAS f32x2*)(L + L_RED + 1024 + 128 * i + col * 4);
        f32x2 gate = {y2.x / (1.0f + __expf(-y2.x)), y2.y / (1.0f + __expf(-y2.y))};
        int off[2];
#pragma unroll
        for (int i = 0; i < 2; ++i) off[i] = WS_SP + f * 8192 + i * 4096 + (lane >> 2) * 256 + 64 * w + 16 * (lane & 3);
        char* dst = p.out + (32 * f + col) * 2;
        keep(gate); keep(off[0]); keep(off[1]); keep(dst);
        wait_tag(rs, WS_HINT + ((ki + 32 * w) & 127) * 128, tag, lane == 0);
        __builtin_amdgcn_s_sleep(SUM_NAP);
        v4u a[2];
        gather_issue(rs, off, a);
        if (!all_valid<true>(a, t2)) gather_retry<true>(rs, off, a, t2);
        float s0 = as_f(a[0].x) + as_f(a[1].x), s1 = as_f(a[0].z) + as_f(a[1].z);
        s0 += ror<4>(s0); s1 += ror<4>(s1);
        s0 += ror<8>(s0); s1 += ror<8>(s1);
        s0 = add32(add16(s0)); s1 = add32(add16(s1));
        if (lane < 4) *(__attribute__((address_space(1))) u32*)dst = pk(bf_lo(xo) + s0 * gate.x, bf_hi(xo) + s1 * gate.y);
        if (ki == 0 && tid == 0) stw64(rs, WS_EPOCH, tag);
    }
}

DI void code_prefetch(u32 r) {
    u64 pc;
    int len_k, len_w;
    asm volatile("s_getpc_b64 %0\n.Lmla_code_begin:\n\ts_mov_b32 %1, .Lmla_code_end_k-.Lmla_code_begin\n\ts_mov_b32 %2, .Lmla_code_end_w-.Lmla_code_begin"
                 : "=s"(pc), "=s"(len_k), "=s"(len_w));
    const int len = max(len_k, len_w);
    const u32 lane = threadIdx.x & 63, w = __builtin_amdgcn_readfirstlane(threadIdx.x >> 6);
    const int off = min((int)(r * 1024 + w * 256 + (lane & 3) * 64), max(len - 4, 0));
    asm volatile("s_mov_b32 m0, %0\n\ts_nop 0\n\tglobal_load_lds_dword %1, %2" :: "s"(131072 + 6144 + 8192 * w), "v"(off), "s"(pc) : "memory");
}

__global__ __launch_bounds__(256, 1) void mla_ffn_decode(Params pa) {
    code_prefetch(blockIdx.x >> 3);
    Params p = pa;
    asm volatile("" : "+s"(p.x), "+s"(p.ckv), "+s"(p.wq), "+s"(p.wuk), "+s"(p.wuv), "+s"(p.wo));
    asm volatile("" : "+s"(p.w1), "+s"(p.b1), "+s"(p.w2), "+s"(p.b2), "+s"(p.out), "+s"(p.ws));
    __shared__ __attribute__((aligned(16))) char smem[163840];
    lds_t L = (lds_t)smem;
    const rsrc_t rs = __builtin_amdgcn_make_buffer_rsrc(p.ws, 0, WS_END, 0x00020000);
    const u32 xcd = blockIdx.x & 7, r = blockIdx.x >> 3;
    if (r >= 16) {
        role_k(p, L, rs, xcd * 16 + (r & 15), xcd);
        asm volatile(".Lmla_code_end_k:");
    } else {
        role_w(p, L, rs, 32 * (xcd & 3) + 2 * (r & 12) + (~xcd & 4) + (r & 3), xcd);
        asm volatile(".Lmla_code_end_w:");
    }
}

}

extern "C" void kernel_launch(void* const* d_in, const int* in_sizes, int n_in,
                              void* d_out, int out_size, void* d_ws, size_t ws_size,
                              hipStream_t stream) {
    mla::Params p;
    p.x = (const char*)d_in[0];
    p.ckv = (const char*)d_in[1];
    p.wq = (const char*)d_in[2];
    p.wuk = (const char*)d_in[3];
    p.wuv = (const char*)d_in[4];
    p.wo = (const char*)d_in[5];
    p.w1 = (const char*)d_in[7];
    p.b1 = (const char*)d_in[8];
    p.w2 = (const char*)d_in[9];
    p.b2 = (const char*)d_in[10];
    p.out = (char*)d_out;
    p.ws = (char*)d_ws;
    mla::mla_ffn_decode<<<256, 256, 0, stream>>>(p);
}
```
